```python
import torch

def _softmax(x, axis=-1):
    return torch.softmax(x, dim=axis)
def _gelu(x):
    return torch.nn.functional.gelu(x, approximate="tanh")
def _silu(x):
    return torch.nn.functional.silu(x)
def _sigmoid(x):
    return torch.sigmoid(x)
def _layernorm(x, axis=-1, eps=1e-5):
    m = x.mean(dim=axis, keepdim=True)
    v = x.var(dim=axis, keepdim=True, unbiased=False)
    return (x - m) / torch.sqrt(v + eps)
def _scatter_add(tgt, idx, src, axis=0):
    return tgt.clone().index_add_(axis, idx, src)
def _segment_sum(src, seg, n):
    out = torch.zeros((n,) + src.shape[1:], dtype=src.dtype)
    return out.index_add_(0, seg, src)
def _pad(x, pw):
    flat = tuple(v for pair in reversed(pw) for v in pair)
    return torch.nn.functional.pad(x, flat)

kernel_name = 'sequential_preLN__grouped_kv_mqa__geglu_ffn__prompt'

def setup_inputs():

    _w = (lambda a: (lambda t: t.to(torch.bfloat16) if t.is_floating_point() else t)(torch.as_tensor(a)))
    def _A_setup():
        B, S, D, H = 1, 2048, 640, 32
        Dh = D // H
        return {
            "x": _w((lambda *s: torch.randn(*s, dtype=torch.bfloat16) * 0.05)(B, S, D)),
            "wq": _w((lambda *s: torch.randn(*s, dtype=torch.bfloat16) * 0.05)(D, H * Dh)),
            "wk": _w((lambda *s: torch.randn(*s, dtype=torch.bfloat16) * 0.05)(D, Dh)),
            "wv": _w((lambda *s: torch.randn(*s, dtype=torch.bfloat16) * 0.05)(D, Dh)),
            "wo": _w((lambda *s: torch.randn(*s, dtype=torch.bfloat16) * 0.05)(H * Dh, D)),
        }
    _A_in = _A_setup()

    def _B_setup():
        B, S, D, F = 1, 2048, 640, 1536
        return {
            "x": _w((lambda *s: torch.randn(*s, dtype=torch.bfloat16) * 0.05)(B, S, D)),
            "w_gate": _w((lambda *s: torch.randn(*s, dtype=torch.bfloat16) * 0.05)(D, F)),
            "w_up": _w((lambda *s: torch.randn(*s, dtype=torch.bfloat16) * 0.05)(D, F)),
            "w_down": _w((lambda *s: torch.randn(*s, dtype=torch.bfloat16) * 0.05)(F, D)),
        }
    _B_in = _B_setup()

    return {
        "A_x": _A_in["x"],
        "A_wq": _A_in["wq"],
        "A_wk": _A_in["wk"],
        "A_wv": _A_in["wv"],
        "A_wo": _A_in["wo"],
        "B_x": _B_in["x"],
        "B_w_gate": _B_in["w_gate"],
        "B_w_up": _B_in["w_up"],
        "B_w_down": _B_in["w_down"],
    }

def reference(A_x, A_wq, A_wk, A_wv, A_wo, B_x, B_w_gate, B_w_up, B_w_down):
    def _A_ref(x, wq, wk, wv, wo):
        B, S, D = x.shape
        H = 32
        Dh = D // H
        q = (lambda x, *d: x.permute(*d))((x @ wq).reshape(B, S, H, Dh), 0, 2, 1, 3)
        k = x @ wk
        v = x @ wv
        scores = torch.einsum("bhqd,bkd->bhqk", q, k) * (Dh ** -0.5)
        p = _softmax(scores, axis=-1)
        o = torch.einsum("bhqk,bkd->bhqd", p, v)
        return (lambda x, *d: x.permute(*d))(o, 0, 2, 1, 3).reshape(B, S, H * Dh) @ wo

    def _B_ref(x, w_gate, w_up, w_down):
        return (_gelu(x @ w_gate) * (x @ w_up)) @ w_down

    x = A_x
    h = x + _A_ref(_layernorm(x, axis=-1), A_wq, A_wk, A_wv, A_wo)
    h2 = h + _B_ref(_layernorm(h, axis=-1), B_w_gate, B_w_up, B_w_down)
    return h2
```

```cpp
#include <hip/hip_runtime.h>
#include <hip/hip_bf16.h>

typedef __attribute__((ext_vector_type(8))) __bf16 bf16x8;
typedef __attribute__((ext_vector_type(4))) __bf16 bf16x4;
typedef __attribute__((ext_vector_type(2))) __bf16 bf16x2;
typedef __attribute__((ext_vector_type(4))) short s16x4;
typedef __attribute__((ext_vector_type(16))) float f32x16;
typedef __attribute__((ext_vector_type(4))) float f32x4;
typedef __attribute__((ext_vector_type(2))) float f32x2;
typedef __attribute__((ext_vector_type(4))) unsigned u32x4;
typedef __attribute__((ext_vector_type(2))) unsigned u32x2;
typedef unsigned short u16;

#define LDS_AS __attribute__((address_space(3)))
#define GLB_AS __attribute__((address_space(1)))

constexpr int S_ = 2048, D_ = 640, F_ = 1536;
constexpr float LN_EPS = 1e-5f;
constexpr float QSCALE = 0.22360679774997896f * 1.4426950408889634f;

constexpr int KVT = 2880;
constexpr int KV_BYTES = 64 * KVT;

constexpr size_t WS_Q = 0;
constexpr size_t WS_KV = WS_Q + (size_t)S_ * D_ * 2;
constexpr size_t WS_KMAX = WS_KV + KV_BYTES;
constexpr size_t WS_O = WS_KMAX + 512;
constexpr size_t WS_H = WS_O + (size_t)S_ * D_ * 2;
constexpr size_t WS_STAT = WS_H + (size_t)S_ * D_ * 2;
constexpr size_t WS_CS = WS_STAT + (size_t)S_ * 8 * 2 * 4;
constexpr size_t WS_ACT = WS_CS + 3072 * 4;

__device__ __forceinline__ unsigned pk_bf16(float a, float b) {
  f32x2 f = {a, b};
  return __builtin_bit_cast(unsigned, __builtin_convertvector(f, bf16x2));
}
__device__ __forceinline__ float bf_lo(unsigned u) { return __builtin_bit_cast(float, u << 16); }
__device__ __forceinline__ float bf_hi(unsigned u) { return __builtin_bit_cast(float, u & 0xffff0000u); }

template <int N> __device__ __forceinline__ void wait_vm() { asm volatile("s_waitcnt vmcnt(%0)" ::"i"(N) : "memory"); }
__device__ __forceinline__ void bar() { __builtin_amdgcn_s_barrier(); }
__device__ __forceinline__ void glds16(const void* g, char* lds) {
  __builtin_amdgcn_global_load_lds((const GLB_AS unsigned*)g, (LDS_AS unsigned*)lds, 16, 0, 0);
}
__device__ __forceinline__ bf16x8 lds_b128(const char* p) { return *(const LDS_AS bf16x8*)(p); }
__device__ __forceinline__ bf16x8 lds_tr_pair(const char* p0, const char* p1) {
  bf16x4 a = __builtin_amdgcn_ds_read_tr16_b64_v4bf16((LDS_AS bf16x4*)p0);
  bf16x4 b = __builtin_amdgcn_ds_read_tr16_b64_v4bf16((LDS_AS bf16x4*)p1);
  return __builtin_shufflevector(a, b, 0, 1, 2, 3, 4, 5, 6, 7);
}
__device__ __forceinline__ f32x4 mfma16(bf16x8 a, bf16x8 b, f32x4 c) {
  return __builtin_amdgcn_mfma_f32_16x16x32_bf16(a, b, c, 0, 0, 0);
}
__device__ __forceinline__ bf16x8 ones8() {
  u32x4 o = {0x3f803f80u, 0x3f803f80u, 0x3f803f80u, 0x3f803f80u};
  return __builtin_bit_cast(bf16x8, o);
}

template <int BM, int NCB> struct StageGeom {
  static constexpr int A_BYTES = BM * 128;
  static constexpr int B_BYTES = 8 * NCB * 256;
  static constexpr int BYTES = A_BYTES + B_BYTES;
  static constexpr int NI_A = BM / 8;
  static constexpr int NI = BYTES / 1024;
};

template <int BM, int NCB, int MAXI>
struct StageSrc {
  const u16* p[MAXI];
  int adv[MAXI];
};

template <int BM, int NCB, int NWAVES, typename ColFn>
__device__ __forceinline__ void make_src(const u16* (&ptr)[(StageGeom<BM, NCB>::NI + NWAVES - 1) / NWAVES],
                                         const u16* A, int lda, int ldw, int wave, int lane, ColFn colptr) {
  using G = StageGeom<BM, NCB>;
  constexpr int MAXI = (G::NI + NWAVES - 1) / NWAVES;
#pragma unroll
  for (int j = 0; j < MAXI; ++j) {
    int i = wave + j * NWAVES;
    if (i < G::NI_A) {
      int row = i * 8 + (lane >> 3);
      int c = (lane & 7) ^ (lane >> 3);
      ptr[j] = A + (size_t)row * lda + c * 8;
    } else {
      int st = (i - G::NI_A) * 4 + (lane >> 4);
      if (st >= 8 * NCB) st = 8 * NCB - 1;
      int kb = st / NCB, cb = st - kb * NCB;
      int rp = (lane & 15) >> 1, half = lane & 1;
      int k = kb * 8 + (rp ^ ((kb & 1) << 2));
      ptr[j] = colptr(cb) + (size_t)k * ldw + half * 8;
    }
  }
}

template <int BM, int NCB, int NWAVES>
__device__ __forceinline__ void issue_stage(const u16* const (&ptr)[(StageGeom<BM, NCB>::NI + NWAVES - 1) / NWAVES],
                                            int stage, int ldw, char* buf, int wave) {
  using G = StageGeom<BM, NCB>;
  constexpr int MAXI = (G::NI + NWAVES - 1) / NWAVES;
#pragma unroll
  for (int j = 0; j < MAXI; ++j) {
    int i = wave + j * NWAVES;
    if (j * NWAVES + NWAVES <= G::NI || i < G::NI) {
      size_t adv = (i < G::NI_A) ? (size_t)stage * 64 : (size_t)stage * 64 * ldw;
      glds16(ptr[j] + adv, buf + i * 1024);
    }
  }
}

template <int BM, int NCB, int NWAVES, int NSTAGES>
__device__ __forceinline__ void wait_stage(int wave) {
  using G = StageGeom<BM, NCB>;
  constexpr int LO = G::NI / NWAVES, REM = G::NI % NWAVES;
  if constexpr (REM == 0) {
    wait_vm<LO * NSTAGES>();
  } else {
    if (wave < REM) wait_vm<(LO + 1) * NSTAGES>();
    else wait_vm<LO * NSTAGES>();
  }
}

constexpr int K1_RING = 7;
constexpr int K1_LDS = 144 * 1024;

template <int WN>
__device__ __forceinline__ void k1_step(const char* Ab, const char* Bb0, const char* Bb1, int ks_off_b, int kstep, int wm,
                                        f32x4 (&acc)[3], f32x4& st, f32x4 (&cs)[3], int a_off) {
  bf16x8 a = lds_b128(Ab + a_off);
  bf16x8 w[3];
#pragma unroll
  for (int j = 0; j < 3; ++j) w[j] = lds_tr_pair(Bb0 + ks_off_b + (WN * 3 + j) * 256, Bb1 + ks_off_b + (WN * 3 + j) * 256);
#pragma unroll
  for (int j = 0; j < 3; ++j) acc[j] = mfma16(w[j], a, acc[j]);
  if (WN == 0) st = mfma16(ones8(), a, st);
  else st = mfma16(a, a, st);
  if ((kstep & 3) == wm) {
#pragma unroll
    for (int j = 0; j < 3; ++j) cs[j] = mfma16(w[j], ones8(), cs[j]);
  }
}

__global__ __launch_bounds__(512, 2) void k1_qkv(const u16* __restrict__ x, const u16* __restrict__ wq,
                                                 const u16* __restrict__ wk, const u16* __restrict__ wv,
                                                 char* __restrict__ ws) {
  extern __shared__ __attribute__((aligned(16))) char smem[];
  const int tid = threadIdx.x, lane = tid & 63;
  const int wave = __builtin_amdgcn_readfirstlane(tid >> 6);
  const int wm = wave & 3, wn = wave >> 2;
  const int b = blockIdx.x;
  const int xcd = b & 7, idx = b >> 3;
  const int rb = xcd * 4 + (idx & 3);
  const int ct = idx >> 2;
  const int row0 = rb * 64;
  const int g = lane >> 4, q = (lane & 15) >> 2, p = lane & 3;
  const int arow = wm * 16 + (lane & 15);

  f32x4 acc[3] = {}, st = {}, cs[3] = {};
  float* scr = (float*)(smem + 142 * 1024);

  if (ct < 7) {
    using G = StageGeom<64, 6>;
    constexpr int MAXI = (G::NI + 7) / 8;
    const u16* ptr[MAXI];
    const int n0 = ct * 96;
    const int ncb = (ct == 6) ? 4 : 6;
    make_src<64, 6, 8>(ptr, x + (size_t)row0 * D_, D_, D_, wave, lane,
                       [&](int cb) { return wq + n0 + (cb < ncb ? cb : 0) * 16; });
#pragma unroll
    for (int s = 0; s < K1_RING - 1; ++s) issue_stage<64, 6, 8>(ptr, s, D_, smem + s * G::BYTES, wave);
    const int boff = g * 6 * 256 + q * 32 + p * 8;
    const int b0 = G::A_BYTES + boff + (g & 1) * 128, b1 = G::A_BYTES + boff + (1 - (g & 1)) * 128;
#pragma unroll
    for (int s = 0; s < 10; ++s) {
      if (s + K1_RING - 2 <= 9) wait_stage<64, 6, 8, K1_RING - 2>(wave);
      else if (s == 5) wait_stage<64, 6, 8, 4>(wave);
      else if (s == 6) wait_stage<64, 6, 8, 3>(wave);
      else if (s == 7) wait_stage<64, 6, 8, 2>(wave);
      else if (s == 8) wait_stage<64, 6, 8, 1>(wave);
      else wait_stage<64, 6, 8, 0>(wave);
      bar();
      if (s + K1_RING - 1 < 10)
        issue_stage<64, 6, 8>(ptr, s + K1_RING - 1, D_, smem + ((s + K1_RING - 1) % K1_RING) * G::BYTES, wave);
      const char* buf = smem + (s % K1_RING) * G::BYTES;
#pragma unroll
      for (int ks = 0; ks < 2; ++ks) {
        int a_off = arow * 128 + (((ks * 4 + g) ^ (arow & 7)) << 4);
        if (wn == 0) k1_step<0>(buf, buf + b0, buf + b1, ks * 4 * 6 * 256, s * 2 + ks, wm, acc, st, cs, a_off);
        else k1_step<1>(buf, buf + b0, buf + b1, ks * 4 * 6 * 256, s * 2 + ks, wm, acc, st, cs, a_off);
      }
    }
  } else {
    constexpr int WOFF = 80 * 1024;
    {
      const u16* ap = x + (size_t)(row0 + (lane >> 3)) * D_ + (((lane & 7) ^ (lane >> 3)) << 3);
#pragma unroll
      for (int s = 0; s < 10; ++s)
        glds16(ap + (size_t)wave * 8 * D_ + s * 64, smem + s * 8192 + wave * 1024);
    }
#pragma unroll
    for (int it = 0; it < 13; ++it) {
      int e = it * 512 + tid;
      if (e < 6400) {
        int k = e / 10, c4 = e - k * 10;
        const u16* src = (c4 < 5) ? (wk + k * 20 + c4 * 4) : (wv + k * 20 + (c4 - 5) * 4);
        u32x2 v = *(const u32x2*)src;
        int kb = k >> 3, cb = c4 >> 2;
        int rp = (k & 7) ^ ((kb & 1) << 2);
        *(LDS_AS u32x2*)(smem + WOFF + (kb * 3 + cb) * 256 + rp * 32 + (c4 & 3) * 8) = v;
      }
    }
    wait_vm<0>();
    __syncthreads();
    if (wn == 0 || true) {
      const int boff = g * 3 * 256 + q * 32 + p * 8;
      const int b0 = WOFF + boff + (g & 1) * 128, b1 = WOFF + boff + (1 - (g & 1)) * 128;
#pragma unroll
      for (int kstep = 0; kstep < 20; ++kstep) {
        int s = kstep >> 1, ks = kstep & 1;
        int a_off = s * 8192 + arow * 128 + (((ks * 4 + g) ^ (arow & 7)) << 4);
        bf16x8 a = lds_b128(smem + a_off);
        if (wn == 0) {
#pragma unroll
          for (int j = 0; j < 3; ++j) {
            bf16x8 w = lds_tr_pair(smem + b0 + kstep * 4 * 3 * 256 + j * 256, smem + b1 + kstep * 4 * 3 * 256 + j * 256);
            acc[j] = mfma16(w, a, acc[j]);
          }
          st = mfma16(ones8(), a, st);
        } else {
          st = mfma16(a, a, st);
          if ((kstep & 3) == wm) {
#pragma unroll
            for (int j = 0; j < 3; ++j) {
              bf16x8 w = lds_tr_pair(smem + b0 + kstep * 4 * 3 * 256 + j * 256, smem + b1 + kstep * 4 * 3 * 256 + j * 256);
              cs[j] = mfma16(w, ones8(), cs[j]);
            }
          }
        }
      }
    }
  }
  if (wn == 0) {
    if (g == 0) scr[wm * 16 + lane] = st[0];
  } else {
    if (g == q) scr[64 + wm * 16 + (lane & 15)] = st[p == 0 ? 0 : p == 1 ? 1 : p == 2 ? 2 : 3];
  }
  if ((lane & 15) == 0) {
    if (ct < 7) {
#pragma unroll
      for (int j = 0; j < 3; ++j) *(LDS_AS f32x4*)(&scr[128 + wm * 96 + (wn * 3 + j) * 16 + g * 4]) = cs[j];
    } else if (wn == 1) {
#pragma unroll
      for (int j = 0; j < 3; ++j) *(LDS_AS f32x4*)(&scr[128 + wm * 96 + j * 16 + g * 4]) = cs[j];
    }
  }
  __syncthreads();
  const float rsum = scr[arow], rsq = scr[64 + arow];
  const float mean = rsum * (1.0f / D_);
  const float var = fmaxf(rsq * (1.0f / D_) - mean * mean, 0.0f);
  const float rstd = rsqrtf(var + LN_EPS);
  const int row = row0 + arow;
  if (ct < 7) {
    u16* Q = (u16*)(ws + WS_Q);
    const int n0 = ct * 96;
#pragma unroll
    for (int j = 0; j < 3; ++j) {
      int cb = wn * 3 + j;
      if (ct == 6 && cb >= 4) continue;
      int nl = cb * 16 + g * 4;
      f32x4 c = *(LDS_AS f32x4*)&scr[128 + nl];
      c += *(LDS_AS f32x4*)&scr[128 + 96 + nl];
      c += *(LDS_AS f32x4*)&scr[128 + 192 + nl];
      c += *(LDS_AS f32x4*)&scr[128 + 288 + nl];
      float v[4];
#pragma unroll
      for (int r = 0; r < 4; ++r) v[r] = (acc[j][r] - mean * c[r]) * (rstd * QSCALE);
      u32x2 o = {pk_bf16(v[0], v[1]), pk_bf16(v[2], v[3])};
      *(u32x2*)(Q + (size_t)row * D_ + n0 + nl) = o;
    }
  } else if (wn == 0) {
    char* kv = ws + WS_KV + (size_t)(row >> 5) * KVT;
    const int kk = row & 31;
    float ksq = 0.f;
#pragma unroll
    for (int j = 0; j < 3; ++j) {
      int nl = j * 16 + g * 4;
      if (nl >= 40) continue;
      f32x4 c = *(LDS_AS f32x4*)&scr[128 + nl];
      c += *(LDS_AS f32x4*)&scr[128 + 96 + nl];
      c += *(LDS_AS f32x4*)&scr[128 + 192 + nl];
      c += *(LDS_AS f32x4*)&scr[128 + 288 + nl];
      float v[4];
#pragma unroll
      for (int r = 0; r < 4; ++r) v[r] = (acc[j][r] - mean * c[r]) * rstd;
      u32x2 o = {pk_bf16(v[0], v[1]), pk_bf16(v[2], v[3])};
      if (nl < 20) {
        float a0 = bf_lo(o[0]), a1 = bf_hi(o[0]), a2 = bf_lo(o[1]), a3 = bf_hi(o[1]);
        ksq += a0 * a0 + a1 * a1 + a2 * a2 + a3 * a3;
        if (nl < 16) {
          *(u32x2*)(kv + ((nl >> 3) * 32 + kk) * 16 + (nl & 7) * 2) = o;
        } else {
          *(u32x2*)(kv + 1024 + kk * 8) = o;
          u32x2 cst = {0x3f803f80u, 0x00003f80u};
          *(u32x2*)(kv + 1024 + 256 + kk * 8) = cst;
        }
      } else {
        int d = nl - 20;
        int s = kk >> 4, k16 = kk & 15;
        int h = (k16 >> 2) & 1, jj = ((k16 >> 3) << 2) | (k16 & 3);
        u16* vt = (u16*)(kv + 1536) + ((s * 2 + h) * 21 + d) * 8 + jj;
        vt[0] = (u16)(o[0] & 0xffff);
        vt[8] = (u16)(o[0] >> 16);
        vt[16] = (u16)(o[1] & 0xffff);
        vt[24] = (u16)(o[1] >> 16);
        if (d == 16) vt[32] = (u16)0x3f80;
      }
    }
    ksq += __shfl_xor(ksq, 16);
    ksq += __shfl_xor(ksq, 32);
#pragma unroll
    for (int o = 1; o < 16; o <<= 1) ksq = fmaxf(ksq, __shfl_xor(ksq, o));
    if (lane == 0) ((float*)(ws + WS_KMAX))[rb * 4 + wm] = ksq;
  }
}

constexpr int AT_STAGE = 8 * KVT;
constexpr int AT_LDS = 4 * AT_STAGE;

__device__ __forceinline__ void split3(float m, unsigned& w0, unsigned& w1) {
  float a = -m;
  unsigned ua = __builtin_bit_cast(unsigned, a) & 0xffff0000u;
  float r1 = a - __builtin_bit_cast(float, ua);
  unsigned ub = __builtin_bit_cast(unsigned, r1) & 0xffff0000u;
  float r2 = r1 - __builtin_bit_cast(float, ub);
  unsigned uc = __builtin_bit_cast(unsigned, r2) & 0xffff0000u;
  w0 = (ua >> 16) | ub;
  w1 = uc >> 16;
}

__device__ __forceinline__ void attn_tile(const char* kt, int lane, int voff, const bf16x8 (&q0)[2], const s16x4 (&q1)[2],
                                          f32x16 (&o)[2]) {
  bf16x8 k0 = lds_b128(kt + lane * 16);
  s16x4 k1 = *(const LDS_AS s16x4*)(kt + 1024 + lane * 8);
  bf16x8 v0 = lds_b128(kt + 1536 + voff);
  bf16x8 v1 = lds_b128(kt + 1536 + 672 + voff);
#pragma unroll
  for (int t = 0; t < 2; ++t) {
    f32x16 z = {};
    f32x16 s = __builtin_amdgcn_mfma_f32_32x32x16_bf16(k0, q0[t], z, 0, 0, 0);
    s = __builtin_amdgcn_mfma_f32_32x32x8bf16_1k(k1, q1[t], s, 0, 0, 0);
    u32x4 p0, p1;
#pragma unroll
    for (int i = 0; i < 4; ++i) {
      p0[i] = pk_bf16(__builtin_amdgcn_exp2f(s[2 * i]), __builtin_amdgcn_exp2f(s[2 * i + 1]));
      p1[i] = pk_bf16(__builtin_amdgcn_exp2f(s[8 + 2 * i]), __builtin_amdgcn_exp2f(s[9 + 2 * i]));
    }
    o[t] = __builtin_amdgcn_mfma_f32_32x32x16_bf16(v0, __builtin_bit_cast(bf16x8, p0), o[t], 0, 0, 0);
    o[t] = __builtin_amdgcn_mfma_f32_32x32x16_bf16(v1, __builtin_bit_cast(bf16x8, p1), o[t], 0, 0, 0);
  }
}

__global__ __launch_bounds__(512, 2) void k2_attn(const u16* __restrict__ wg, const u16* __restrict__ wu,
                                                  char* __restrict__ ws) {
  extern __shared__ __attribute__((aligned(16))) char smem[];
  const int tid = threadIdx.x, lane = tid & 63;
  const int wave = __builtin_amdgcn_readfirstlane(tid >> 6);
  const int grp = wave >> 2, wq = wave & 3;
  const int r = lane & 31, h = lane >> 5;
  const char* kvg = ws + WS_KV;
  const int pos0 = blockIdx.x * 8 + wq * 2;

  auto issue = [&](int st, int buf) {
#pragma unroll
    for (int it = 0; it < 6; ++it) {
      int c0 = it * 512 + wave * 64;
      int c = c0 + lane;
      if (c0 < 2880) {
        int gsel = c0 >= 1440;
        (void)gsel;
        int gg = c >= 1440;
        int cc = c - gg * 1440;
        const char* src = kvg + (size_t)(gg * 4 + st) * AT_STAGE + cc * 16;
        if (c < 2880) glds16(src, smem + buf * 2 * AT_STAGE + c0 * 16);
      }
    }
  };
  issue(0, 0);

  {
    const int c0 = blockIdx.x * 12;
    const u16* w = (c0 < F_) ? (wg + c0) : (wu + (c0 - F_));
    float a[12];
#pragma unroll
    for (int i = 0; i < 12; ++i) a[i] = 0.f;
#pragma unroll
    for (int it = 0; it < 2; ++it) {
      int k = tid + it * 512;
      if (k < D_) {
        const u32x2* s = (const u32x2*)(w + (size_t)k * F_);
        u32x2 v0 = s[0], v1 = s[1], v2 = s[2];
        a[0] += bf_lo(v0[0]); a[1] += bf_hi(v0[0]); a[2] += bf_lo(v0[1]); a[3] += bf_hi(v0[1]);
        a[4] += bf_lo(v1[0]); a[5] += bf_hi(v1[0]); a[6] += bf_lo(v1[1]); a[7] += bf_hi(v1[1]);
        a[8] += bf_lo(v2[0]); a[9] += bf_hi(v2[0]); a[10] += bf_lo(v2[1]); a[11] += bf_hi(v2[1]);
      }
    }
#pragma unroll
    for (int i = 0; i < 12; ++i) {
#pragma unroll
      for (int o = 1; o < 64; o <<= 1) a[i] += __shfl_xor(a[i], o);
    }
    float* red = (float*)(smem + AT_LDS);
    if (lane == 0) {
#pragma unroll
      for (int i = 0; i < 12; ++i) red[wave * 12 + i] = a[i];
    }
  }

  float kmax2;
  {
    const float* km = (const float*)(ws + WS_KMAX);
    float m = fmaxf(km[lane], km[lane + 64]);
#pragma unroll
    for (int o = 1; o < 64; o <<= 1) m = fmaxf(m, __shfl_xor(m, o));
    kmax2 = m;
  }
  bf16x8 q0[2];
  s16x4 q1[2];
  u32x2 qtail[2];
  float mshift[2];
  const u16* Qs = (const u16*)(ws + WS_Q);
#pragma unroll
  for (int t = 0; t < 2; ++t) {
    const u16* qp = Qs + (size_t)(pos0 + t) * D_ + r * 20;
    u32x2 a = *(const u32x2*)(qp + 8 * h), b2 = *(const u32x2*)(qp + 8 * h + 4);
    u32x4 qq = {a[0], a[1], b2[0], b2[1]};
    q0[t] = __builtin_bit_cast(bf16x8, qq);
    u32x2 tl = *(const u32x2*)(qp + 16);
    qtail[t] = tl;
    float ss = 0.f;
#pragma unroll
    for (int i = 0; i < 4; ++i) { float x0 = bf_lo(qq[i]), x1 = bf_hi(qq[i]); ss += x0 * x0 + x1 * x1; }
    if (h == 0) {
#pragma unroll
      for (int i = 0; i < 2; ++i) { float x0 = bf_lo(tl[i]), x1 = bf_hi(tl[i]); ss += x0 * x0 + x1 * x1; }
    }
    ss += __shfl_xor(ss, 32);
    mshift[t] = sqrtf(ss * kmax2) * 1.0001f;
  }

  const int voff = (h * 21 + (r < 21 ? r : 20)) * 16;
  f32x16 o[2];
  float* xch = (float*)smem;
  int* flag = (int*)(smem + AT_LDS + 512);

  for (int attempt = 0;; ++attempt) {
#pragma unroll
    for (int t = 0; t < 2; ++t) {
      unsigned w0, w1;
      split3(mshift[t], w0, w1);
      u32x2 f = (h == 0) ? qtail[t] : u32x2{w0, w1};
      q1[t] = __builtin_bit_cast(s16x4, f);
#pragma unroll
      for (int i = 0; i < 16; ++i) o[t][i] = 0.f;
    }
    if (attempt) issue(0, 0);
#pragma unroll 1
    for (int st = 0; st < 4; ++st) {
      wait_vm<0>();
      bar();
      if (st < 3) issue(st + 1, (st + 1) & 1);
      const char* kb = smem + (st & 1) * 2 * AT_STAGE + grp * AT_STAGE;
#pragma unroll
      for (int kt = 0; kt < 8; ++kt) attn_tile(kb + kt * KVT, lane, voff, q0, q1, o);
    }
    __syncthreads();
    if (grp == 1) {
#pragma unroll
      for (int t = 0; t < 2; ++t)
#pragma unroll
        for (int i = 0; i < 12; ++i) xch[((wq * 2 + t) * 12 + i) * 64 + lane] = o[t][i];
    }
    if (tid == 0) *flag = 0;
    __syncthreads();
    bool bad = false;
    float linv[2];
    if (grp == 0) {
#pragma unroll
      for (int t = 0; t < 2; ++t) {
#pragma unroll
        for (int i = 0; i < 12; ++i) o[t][i] += xch[((wq * 2 + t) * 12 + i) * 64 + lane];
        float l = __shfl(o[t][8], 32 + r);
        bad |= !(l >= 5.421010862427522e-20f);
        linv[t] = 1.0f / l;
      }
      if (attempt == 0 && bad) *flag = 1;
    }
    __syncthreads();
    if (attempt == 1 || *flag == 0) {
      if (grp == 0) {
        u16* O = (u16*)(ws + WS_O);
#pragma unroll
        for (int t = 0; t < 2; ++t) {
          u16* op = O + (size_t)(pos0 + t) * D_ + r * 20 + 4 * h;
#pragma unroll
          for (int gq = 0; gq < 3; ++gq) {
            if (gq == 2 && h == 1) continue;
            u32x2 v = {pk_bf16(o[t][4 * gq] * linv[t], o[t][4 * gq + 1] * linv[t]),
                       pk_bf16(o[t][4 * gq + 2] * linv[t], o[t][4 * gq + 3] * linv[t])};
            *(u32x2*)(op + 8 * gq) = v;
          }
        }
      }
      break;
    }
    __syncthreads();
#pragma unroll 1
    for (int t = 0; t < 2; ++t) {
      const u16* qp = Qs + (size_t)(pos0 + t) * D_ + r * 20;
      float qf[20];
#pragma unroll
      for (int i = 0; i < 10; ++i) { unsigned u = *(const unsigned*)(qp + 2 * i); qf[2 * i] = bf_lo(u); qf[2 * i + 1] = bf_hi(u); }
      float mx = -INFINITY;
#pragma unroll 1
      for (int kk = 0; kk < 512; ++kk) {
        int key = grp * 1024 + h * 512 + kk;
        const char* t0 = kvg + (size_t)(key >> 5) * KVT;
        int kr = key & 31;
        u32x4 a = *(const u32x4*)(t0 + kr * 16), b2 = *(const u32x4*)(t0 + 512 + kr * 16);
        u32x2 c = *(const u32x2*)(t0 + 1024 + kr * 8);
        float sacc = 0.f;
#pragma unroll
        for (int i = 0; i < 4; ++i) {
          sacc += qf[2 * i] * bf_lo(a[i]) + qf[2 * i + 1] * bf_hi(a[i]);
          sacc += qf[8 + 2 * i] * bf_lo(b2[i]) + qf[9 + 2 * i] * bf_hi(b2[i]);
        }
        sacc += qf[16] * bf_lo(c[0]) + qf[17] * bf_hi(c[0]) + qf[18] * bf_lo(c[1]) + qf[19] * bf_hi(c[1]);
        mx = fmaxf(mx, sacc);
      }
      mx = fmaxf(mx, __shfl_xor(mx, 32));
      if (h == 0) xch[(grp * 8 + wq * 2 + t) * 32 + r] = mx;
    }
    __syncthreads();
#pragma unroll
    for (int t = 0; t < 2; ++t) mshift[t] = fmaxf(xch[(wq * 2 + t) * 32 + r], xch[(8 + wq * 2 + t) * 32 + r]);
    __syncthreads();
  }
  if (tid < 12) {
    const float* red = (const float*)(smem + AT_LDS);
    float s = 0.f;
#pragma unroll
    for (int w = 0; w < 8; ++w) s += red[w * 12 + tid];
    ((float*)(ws + WS_CS))[blockIdx.x * 12 + tid] = s;
  }
}

constexpr int G2_RING = 8;
constexpr int G2_LDS = G2_RING * 18 * 1024;

template <int K, bool STATS>
__global__ __launch_bounds__(512, 2) void k_gemm80(const u16* __restrict__ A, const u16* __restrict__ W,
                                                   const u16* __restrict__ res, u16* __restrict__ out,
                                                   float* __restrict__ stats) {
  extern __shared__ __attribute__((aligned(16))) char smem[];
  using G = StageGeom<64, 5>;
  constexpr int NS = K / 64;
  constexpr int MAXI = (G::NI + 7) / 8;
  const int tid = threadIdx.x, lane = tid & 63;
  const int wave = __builtin_amdgcn_readfirstlane(tid >> 6);
  const int wm = wave & 3, wn = wave >> 2;
  const int b = blockIdx.x;
  const int xcd = b & 7, idx = b >> 3;
  const int rb = xcd * 4 + (idx & 3), ct = idx >> 2;
  const int row0 = rb * 64, n0 = ct * 80;
  const int g = lane >> 4, q = (lane & 15) >> 2, p = lane & 3;
  const int arow = wm * 16 + (lane & 15);
  const int row = row0 + arow;

  u32x2 rv[3];
#pragma unroll
  for (int j = 0; j < 3; ++j) {
    int cb = wn * 3 + j;
    rv[j] = (cb < 5) ? *(const u32x2*)(res + (size_t)row * D_ + n0 + cb * 16 + g * 4) : u32x2{0u, 0u};
  }
  const u16* ptr[MAXI];
  make_src<64, 5, 8>(ptr, A + (size_t)row0 * K, K, D_, wave, lane, [&](int cb) { return W + n0 + cb * 16; });
#pragma unroll
  for (int s = 0; s < G2_RING - 1; ++s) issue_stage<64, 5, 8>(ptr, s, D_, smem + s * G::BYTES, wave);
  const int boff = G::A_BYTES + g * 5 * 256 + q * 32 + p * 8 + wn * 3 * 256;
  const int b0 = boff + (g & 1) * 128, b1 = boff + (1 - (g & 1)) * 128;
  f32x4 acc[3] = {};
  auto compute = [&](const char* buf) {
#pragma unroll
    for (int ks = 0; ks < 2; ++ks) {
      bf16x8 a = lds_b128(buf + arow * 128 + (((ks * 4 + g) ^ (arow & 7)) << 4));
      bf16x8 w0 = lds_tr_pair(buf + b0 + ks * 5120, buf + b1 + ks * 5120);
      bf16x8 w1 = lds_tr_pair(buf + b0 + ks * 5120 + 256, buf + b1 + ks * 5120 + 256);
      acc[0] = mfma16(w0, a, acc[0]);
      acc[1] = mfma16(w1, a, acc[1]);
      if (wn == 0) {
        bf16x8 w2 = lds_tr_pair(buf + b0 + ks * 5120 + 512, buf + b1 + ks * 5120 + 512);
        acc[2] = mfma16(w2, a, acc[2]);
      }
    }
  };
  constexpr int NMAIN = NS - (G2_RING - 1);
  int slot = 0;
#pragma unroll 1
  for (int s = 0; s < NMAIN; ++s) {
    wait_stage<64, 5, 8, G2_RING - 2>(wave);
    bar();
    int ns = slot == 0 ? G2_RING - 1 : slot - 1;
    issue_stage<64, 5, 8>(ptr, s + G2_RING - 1, D_, smem + ns * G::BYTES, wave);
    compute(smem + slot * G::BYTES);
    slot = slot == G2_RING - 1 ? 0 : slot + 1;
  }
#define DRAIN(R)                                   \
  wait_stage<64, 5, 8, R>(wave);                   \
  bar();                                           \
  compute(smem + slot * G::BYTES);                 \
  slot = slot == G2_RING - 1 ? 0 : slot + 1;
  DRAIN(6) DRAIN(5) DRAIN(4) DRAIN(3) DRAIN(2) DRAIN(1) DRAIN(0)
#undef DRAIN

  float sum = 0.f;
  float hv[3][4];
#pragma unroll
  for (int j = 0; j < 3; ++j) {
    int cb = wn * 3 + j;
    if (cb < 5) {
      float v0 = acc[j][0] + bf_lo(rv[j][0]), v1 = acc[j][1] + bf_hi(rv[j][0]);
      float v2 = acc[j][2] + bf_lo(rv[j][1]), v3 = acc[j][3] + bf_hi(rv[j][1]);
      u32x2 o = {pk_bf16(v0, v1), pk_bf16(v2, v3)};
      *(u32x2*)(out + (size_t)row * D_ + n0 + cb * 16 + g * 4) = o;
      if (STATS) {
        hv[j][0] = bf_lo(o[0]); hv[j][1] = bf_hi(o[0]); hv[j][2] = bf_lo(o[1]); hv[j][3] = bf_hi(o[1]);
        sum += hv[j][0] + hv[j][1] + hv[j][2] + hv[j][3];
      }
    } else if (STATS) {
      hv[j][0] = hv[j][1] = hv[j][2] = hv[j][3] = 0.f;
    }
  }
  if (STATS) {
    float* scr = (float*)smem;
    sum += __shfl_xor(sum, 16);
    sum += __shfl_xor(sum, 32);
    __syncthreads();
    if (g == 0) scr[wn * 64 + arow] = sum;
    __syncthreads();
    const float tot = scr[arow] + scr[64 + arow];
    const float mu = tot * (1.0f / 80.0f);
    float m2 = 0.f;
#pragma unroll
    for (int j = 0; j < 3; ++j) {
      if (wn * 3 + j < 5) {
#pragma unroll
        for (int r = 0; r < 4; ++r) { float d = hv[j][r] - mu; m2 += d * d; }
      }
    }
    m2 += __shfl_xor(m2, 16);
    m2 += __shfl_xor(m2, 32);
    if (g == 0) scr[128 + wn * 64 + arow] = m2;
    __syncthreads();
    if (wn == 0 && g == 0) {
      f32x2 o = {tot, scr[128 + arow] + scr[192 + arow]};
      *(f32x2*)(stats + ((size_t)row * 8 + ct) * 2) = o;
    }
  }
}

constexpr int G3_RING = 3;
constexpr int G3_LDS = G3_RING * 40 * 1024 + 2048;

__global__ __launch_bounds__(512, 2) void k4_ffn_up(const u16* __restrict__ Hh, const u16* __restrict__ wg,
                                                    const u16* __restrict__ wu, const float* __restrict__ stats,
                                                    const float* __restrict__ cs, u16* __restrict__ act) {
  extern __shared__ __attribute__((aligned(16))) char smem[];
  using G = StageGeom<128, 12>;
  constexpr int MAXI = G::NI / 8;
  const int tid = threadIdx.x, lane = tid & 63;
  const int wave = __builtin_amdgcn_readfirstlane(tid >> 6);
  const int wm = wave & 3, wn = wave >> 2;
  const int b = blockIdx.x;
  const int xcd = b & 7, idx = b >> 3;
  const int rb = (xcd & 1) * 8 + (idx & 7);
  const int ct = (xcd >> 1) * 4 + (idx >> 3);
  const int row0 = rb * 128, n0 = ct * 96;
  const int g = lane >> 4, q = (lane & 15) >> 2, p = lane & 3;

  const u16* ptr[MAXI];
  make_src<128, 12, 8>(ptr, Hh + (size_t)row0 * D_, D_, F_, wave, lane,
                       [&](int cb) { return (cb < 6 ? wg + cb * 16 : wu + (cb - 6) * 16) + n0; });
#pragma unroll
  for (int s = 0; s < G3_RING - 1; ++s) issue_stage<128, 12, 8>(ptr, s, F_, smem + s * G::BYTES, wave);

  float* scr = (float*)(smem + G3_RING * G::BYTES);
  float mean_r = 0.f, rstd_r = 0.f;
  if (tid < 128) {
    const f32x4* sp = (const f32x4*)(stats + (size_t)(row0 + tid) * 16);
    f32x4 s0 = sp[0], s1 = sp[1], s2 = sp[2], s3 = sp[3];
    float tot = s0[0] + s0[2] + s1[0] + s1[2] + s2[0] + s2[2] + s3[0] + s3[2];
    float mean = tot * (1.0f / D_);
    float m2 = s0[1] + s0[3] + s1[1] + s1[3] + s2[1] + s2[3] + s3[1] + s3[3];
    float c = 0.f, d;
    d = s0[0] * (1.f / 80.f) - mean; c += d * d; d = s0[2] * (1.f / 80.f) - mean; c += d * d;
    d = s1[0] * (1.f / 80.f) - mean; c += d * d; d = s1[2] * (1.f / 80.f) - mean; c += d * d;
    d = s2[0] * (1.f / 80.f) - mean; c += d * d; d = s2[2] * (1.f / 80.f) - mean; c += d * d;
    d = s3[0] * (1.f / 80.f) - mean; c += d * d; d = s3[2] * (1.f / 80.f) - mean; c += d * d;
    float var = (m2 + 80.f * c) * (1.0f / D_);
    mean_r = mean;
    rstd_r = rsqrtf(var + LN_EPS);
  }

  const int boff = G::A_BYTES + g * 12 * 256 + q * 32 + p * 8 + wn * 3 * 256;
  const int b0 = boff + (g & 1) * 128, b1 = boff + (1 - (g & 1)) * 128;
  const int ar0 = wm * 32 + (lane & 15);
  f32x4 ag[2][3] = {}, au[2][3] = {};
#pragma unroll
  for (int s = 0; s < 10; ++s) {
    if (s < 9) wait_stage<128, 12, 8, 1>(wave);
    else wait_stage<128, 12, 8, 0>(wave);
    bar();
    if (s + 2 < 10) issue_stage<128, 12, 8>(ptr, s + 2, F_, smem + ((s + 2) % 3) * G::BYTES, wave);
    const char* buf = smem + (s % 3) * G::BYTES;
#pragma unroll
    for (int ks = 0; ks < 2; ++ks) {
      bf16x8 a[2];
#pragma unroll
      for (int mi = 0; mi < 2; ++mi) {
        int ar = ar0 + mi * 16;
        a[mi] = lds_b128(buf + ar * 128 + (((ks * 4 + g) ^ (ar & 7)) << 4));
      }
#pragma unroll
      for (int j = 0; j < 3; ++j) {
        bf16x8 w_g = lds_tr_pair(buf + b0 + ks * 12288 + j * 256, buf + b1 + ks * 12288 + j * 256);
        bf16x8 w_u = lds_tr_pair(buf + b0 + ks * 12288 + (6 + j) * 256, buf + b1 + ks * 12288 + (6 + j) * 256);
#pragma unroll
        for (int mi = 0; mi < 2; ++mi) {
          ag[mi][j] = mfma16(w_g, a[mi], ag[mi][j]);
          au[mi][j] = mfma16(w_u, a[mi], au[mi][j]);
        }
      }
    }
  }
  if (tid < 128) { scr[tid * 2] = mean_r; scr[tid * 2 + 1] = rstd_r; }
  __syncthreads();
#pragma unroll
  for (int mi = 0; mi < 2; ++mi) {
    const int ar = ar0 + mi * 16;
    const f32x2 ms = *(LDS_AS f32x2*)&scr[ar * 2];
    const float rstd = ms[1], nm = -ms[0] * ms[1];
#pragma unroll
    for (int j = 0; j < 3; ++j) {
      const int n = n0 + (wn * 3 + j) * 16 + g * 4;
      const f32x4 cg = *(const f32x4*)(cs + n), cu = *(const f32x4*)(cs + F_ + n);
      float o[4];
#pragma unroll
      for (int r = 0; r < 4; ++r) {
        float gv = ag[mi][j][r] * rstd + nm * cg[r];
        float uv = au[mi][j][r] * rstd + nm * cu[r];
        float t = gv * gv * (0.044715f * 1.5957691216057308f * 1.4426950408889634f) + (1.5957691216057308f * 1.4426950408889634f);
        float e = __builtin_amdgcn_exp2f(-gv * t);
        o[r] = gv * uv * __builtin_amdgcn_rcpf(1.0f + e);
      }
      u32x2 v = {pk_bf16(o[0], o[1]), pk_bf16(o[2], o[3])};
      *(u32x2*)(act + (size_t)(row0 + ar) * F_ + n) = v;
    }
  }
}

extern "C" void kernel_launch(void* const* d_in, const int* in_sizes, int n_in, void* d_out, int out_size,
                              void* d_ws, size_t ws_size, hipStream_t stream) {
  const u16* x = (const u16*)d_in[0];
  const u16* wq = (const u16*)d_in[1];
  const u16* wk = (const u16*)d_in[2];
  const u16* wv = (const u16*)d_in[3];
  const u16* wo = (const u16*)d_in[4];
  const u16* wg = (const u16*)d_in[6];
  const u16* wu = (const u16*)d_in[7];
  const u16* wd = (const u16*)d_in[8];
  char* ws = (char*)d_ws;
  u16* out = (u16*)d_out;

  (void)hipFuncSetAttribute((const void*)k1_qkv, hipFuncAttributeMaxDynamicSharedMemorySize, K1_LDS);
  (void)hipFuncSetAttribute((const void*)k2_attn, hipFuncAttributeMaxDynamicSharedMemorySize, AT_LDS + 1024);
  (void)hipFuncSetAttribute((const void*)k_gemm80<640, true>, hipFuncAttributeMaxDynamicSharedMemorySize, G2_LDS);
  (void)hipFuncSetAttribute((const void*)k_gemm80<1536, false>, hipFuncAttributeMaxDynamicSharedMemorySize, G2_LDS);
  (void)hipFuncSetAttribute((const void*)k4_ffn_up, hipFuncAttributeMaxDynamicSharedMemorySize, G3_LDS);

  k1_qkv<<<256, 512, K1_LDS, stream>>>(x, wq, wk, wv, ws);
  k2_attn<<<256, 512, AT_LDS + 1024, stream>>>(wg, wu, ws);
  k_gemm80<640, true><<<256, 512, G2_LDS, stream>>>((const u16*)(ws + WS_O), wo, x, (u16*)(ws + WS_H),
                                                    (float*)(ws + WS_STAT));
  k4_ffn_up<<<256, 512, G3_LDS, stream>>>((const u16*)(ws + WS_H), wg, wu, (const float*)(ws + WS_STAT),
                                          (const float*)(ws + WS_CS), (u16*)(ws + WS_ACT));
  k_gemm80<1536, false><<<256, 512, G2_LDS, stream>>>((const u16*)(ws + WS_ACT), wd, (const u16*)(ws + WS_H), out,
                                                      (float*)(ws + WS_STAT));
}
```

```cpp
#include <hip/hip_runtime.h>
#include <hip/hip_bf16.h>

typedef __attribute__((ext_vector_type(8))) __bf16 bf16x8;
typedef __attribute__((ext_vector_type(4))) __bf16 bf16x4;
typedef __attribute__((ext_vector_type(2))) __bf16 bf16x2;
typedef __attribute__((ext_vector_type(4))) short s16x4;
typedef __attribute__((ext_vector_type(16))) float f32x16;
typedef __attribute__((ext_vector_type(4))) float f32x4;
typedef __attribute__((ext_vector_type(2))) float f32x2;
typedef __attribute__((ext_vector_type(4))) unsigned u32x4;
typedef __attribute__((ext_vector_type(2))) unsigned u32x2;
typedef unsigned short u16;

#define LDS_AS __attribute__((address_space(3)))
#define GLB_AS __attribute__((address_space(1)))

constexpr int S_ = 2048, D_ = 640, F_ = 1536;
constexpr float LN_EPS = 1e-5f;
constexpr float QSCALE = 0.22360679774997896f * 1.4426950408889634f;

constexpr int KVT = 2880;
constexpr int KV_BYTES = 64 * KVT;

constexpr size_t WS_Q = 0;
constexpr size_t WS_KV = WS_Q + (size_t)S_ * D_ * 2;
constexpr size_t WS_KMAX = WS_KV + KV_BYTES;
constexpr size_t WS_O = WS_KMAX + 512;
constexpr size_t WS_H = WS_O + (size_t)S_ * D_ * 2;
constexpr size_t WS_STAT = WS_H + (size_t)S_ * D_ * 2;
constexpr size_t WS_CS = WS_STAT + (size_t)S_ * 8 * 2 * 4;
constexpr size_t WS_ACT = WS_CS + 3072 * 4;

__device__ __forceinline__ unsigned pk_bf16(float a, float b) {
  f32x2 f = {a, b};
  return __builtin_bit_cast(unsigned, __builtin_convertvector(f, bf16x2));
}
__device__ __forceinline__ float bf_lo(unsigned u) { return __builtin_bit_cast(float, u << 16); }
__device__ __forceinline__ float bf_hi(unsigned u) { return __builtin_bit_cast(float, u & 0xffff0000u); }

#define STAMP(ws_, kid, i)
template <int N> __device__ __forceinline__ void wait_vm() { asm volatile("s_waitcnt vmcnt(%0)" ::"i"(N) : "memory"); }
__device__ __forceinline__ void bar() { __builtin_amdgcn_s_barrier(); }
__device__ __forceinline__ void glds16s(unsigned voff, const void* sbase, unsigned lds) {
  asm volatile("s_mov_b32 m0, %2\n\ts_nop 3\n\tglobal_load_lds_dwordx4 %0, %1" ::"v"(voff), "s"(sbase), "s"(lds)
               : "memory");
}
__device__ __forceinline__ unsigned lds_addr(const char* p) { return (unsigned)(size_t)(const LDS_AS char*)p; }
__device__ __forceinline__ void lds_fence() { asm volatile("s_waitcnt lgkmcnt(0)" ::: "memory"); }
__device__ __forceinline__ bf16x8 lds_b128(const char* p) { return *(const LDS_AS bf16x8*)(p); }
__device__ __forceinline__ bf16x8 lds_tr_pair(const char* p0, const char* p1) {
  bf16x4 a = __builtin_amdgcn_ds_read_tr16_b64_v4bf16((LDS_AS bf16x4*)p0);
  bf16x4 b = __builtin_amdgcn_ds_read_tr16_b64_v4bf16((LDS_AS bf16x4*)p1);
  return __builtin_shufflevector(a, b, 0, 1, 2, 3, 4, 5, 6, 7);
}
__device__ __forceinline__ f32x4 mfma16(bf16x8 a, bf16x8 b, f32x4 c) {
  return __builtin_amdgcn_mfma_f32_16x16x32_bf16(a, b, c, 0, 0, 0);
}
__device__ __forceinline__ bf16x8 ones8() {
  u32x4 o = {0x3f803f80u, 0x3f803f80u, 0x3f803f80u, 0x3f803f80u};
  return __builtin_bit_cast(bf16x8, o);
}

__device__ __forceinline__ unsigned voff_a(int row, int lane, int lda) {
  return (unsigned)((row + (lane >> 3)) * lda + (((lane & 7) ^ (lane >> 3)) << 3)) * 2u;
}
__device__ __forceinline__ unsigned voff_g64(int kb, int lane, int ldw, int c64) {
  int r = lane >> 3, pos = lane & 7;
  int u = (pos >> 1) ^ (((r >> 1) & 1) | ((kb & 1) << 1));
  return (unsigned)((kb * 8 + r) * ldw + c64 + u * 16 + (pos & 1) * 8) * 2u;
}
__device__ __forceinline__ unsigned voff_g32(int kbp, int lane, int ldw, int c32) {
  int kb = kbp * 2 + (lane >> 5), r = (lane >> 2) & 7, pos = lane & 3;
  int u = (pos >> 1) ^ (kb & 1);
  return (unsigned)((kb * 8 + r) * ldw + c32 + u * 16 + (pos & 1) * 8) * 2u;
}
__device__ __forceinline__ int roff_g64(int u, int lane) {
  int g = lane >> 4, q = (lane & 15) >> 2, p = lane & 3;
  return g * 1024 + q * 128 + ((u ^ (((q >> 1) & 1) | ((g & 1) << 1))) << 5) + p * 8;
}
__device__ __forceinline__ int roff_g32(int v, int lane) {
  int g = lane >> 4, q = (lane & 15) >> 2, p = lane & 3;
  return g * 512 + q * 64 + ((v ^ (g & 1)) << 5) + p * 8;
}
__device__ __forceinline__ bf16x8 rd_g64(const char* p, int ks) { return lds_tr_pair(p + ks * 4096, p + ks * 4096 + 512); }
__device__ __forceinline__ bf16x8 rd_g32(const char* p, int ks) { return lds_tr_pair(p + ks * 2048, p + ks * 2048 + 256); }

constexpr int ST64 = 20 * 1024;

template <int NS, int RING, int NPW, bool HAS32, bool QKV>
__device__ __forceinline__ void gemm64_loop(char* smem, const char* abase, const char* wbase, int ldw2, unsigned va,
                                            unsigned v64, unsigned v32, int wave, int lane, f32x4 (&acc)[3], f32x4& st,
                                            f32x4 (&cs)[3]) {
  const int wm = wave & 3, wn = wave >> 2, g = lane >> 4;
  const unsigned l0 = lds_addr(smem);
  const int arow = wm * 16 + (lane & 15);
  const int ao0 = arow * 128 + ((g ^ (arow & 7)) << 4), ao1 = arow * 128 + (((4 + g) ^ (arow & 7)) << 4);
  const int wo0 = 8192 + roff_g64(wn * 2, lane), wo1 = 8192 + roff_g64(wn * 2 + 1, lane), wo2 = 16384 + roff_g32(wn, lane);
  auto issue = [&](int s, int slot) {
    const unsigned d = l0 + slot * ST64 + wave * 1024;
    const char* w = wbase + (size_t)s * 64 * ldw2;
    glds16s(va, abase + s * 128, d);
    glds16s(v64, w, d + 8192);
    if (NPW == 3) glds16s(v32, w, d + 16384);
  };
  auto compute = [&](const char* buf, int s) {
#pragma unroll
    for (int ks = 0; ks < 2; ++ks) {
      bf16x8 a = lds_b128(buf + (ks ? ao1 : ao0));
      bf16x8 w0 = rd_g64(buf + wo0, ks), w1 = rd_g64(buf + wo1, ks), w2;
      if (HAS32) w2 = rd_g32(buf + wo2, ks);
      acc[0] = mfma16(w0, a, acc[0]);
      acc[1] = mfma16(w1, a, acc[1]);
      if (HAS32) acc[2] = mfma16(w2, a, acc[2]);
      if (QKV) {
        if (wn == 0) st = mfma16(ones8(), a, st);
        else st = mfma16(a, a, st);
        if (((s * 2 + ks) & 3) == wm) {
          cs[0] = mfma16(w0, ones8(), cs[0]);
          cs[1] = mfma16(w1, ones8(), cs[1]);
          if (HAS32) cs[2] = mfma16(w2, ones8(), cs[2]);
        }
      }
    }
  };
#pragma unroll
  for (int s = 0; s < RING - 1; ++s) issue(s, s);
  constexpr int NMAIN = NS - (RING - 1);
  int slot = 0;
#pragma unroll 1
  for (int s = 0; s < NMAIN; ++s) {
    wait_vm<NPW*(RING - 2)>();
    bar();
    issue(s + RING - 1, slot == 0 ? RING - 1 : slot - 1);
    compute(smem + slot * ST64, s);
    slot = slot == RING - 1 ? 0 : slot + 1;
  }
#pragma unroll
  for (int r = RING - 2; r >= 0; --r) {
    if (r == 6) wait_vm<NPW * 6>();
    else if (r == 5) wait_vm<NPW * 5>();
    else if (r == 4) wait_vm<NPW * 4>();
    else if (r == 3) wait_vm<NPW * 3>();
    else if (r == 2) wait_vm<NPW * 2>();
    else if (r == 1) wait_vm<NPW * 1>();
    else wait_vm<0>();
    bar();
    compute(smem + slot * ST64, NS - 1 - r);
    slot = slot == RING - 1 ? 0 : slot + 1;
  }
}

template <int NS, int RING, bool QKV>
__device__ __forceinline__ void gemm64(char* smem, const u16* A, int lda, const u16* W, int ldw, int ct, int wave, int lane,
                                       f32x4 (&acc)[3], f32x4& st, f32x4 (&cs)[3]) {
  const int c64 = (ct & 1) ? 32 : 0, c32 = (ct & 1) ? 0 : 64;
  const unsigned va = voff_a(wave * 8, lane, lda);
  const unsigned v64 = voff_g64(wave, lane, ldw, c64), v32 = voff_g32(wave & 3, lane, ldw, c32);
  const char* ab = (const char*)A;
  const char* wb = (const char*)W;
  if (ct == 6) gemm64_loop<NS, RING, 2, false, QKV>(smem, ab, wb, ldw * 2, va, v64, v32, wave, lane, acc, st, cs);
  else if (wave < 4) gemm64_loop<NS, RING, 3, true, QKV>(smem, ab, wb, ldw * 2, va, v64, v32, wave, lane, acc, st, cs);
  else gemm64_loop<NS, RING, 2, true, QKV>(smem, ab, wb, ldw * 2, va, v64, v32, wave, lane, acc, st, cs);
}
__device__ __forceinline__ int acc_col(int ct, int wn, int j) {
  return j < 2 ? ((ct & 1) ? 32 : 0) + (wn * 2 + j) * 16 : ((ct & 1) ? 0 : 64) + wn * 16;
}

constexpr int K1_RING = 7;
constexpr int K1_LDS = 144 * 1024;

__global__ __launch_bounds__(512, 2) void k1_qkv(const u16* __restrict__ x, const u16* __restrict__ wq,
                                                 const u16* __restrict__ wk, const u16* __restrict__ wv,
                                                 char* __restrict__ ws) {
  extern __shared__ __attribute__((aligned(16))) char smem[];
  const int tid = threadIdx.x, lane = tid & 63;
  const int wave = __builtin_amdgcn_readfirstlane(tid >> 6);
  const int wm = wave & 3, wn = wave >> 2;
  const int b = blockIdx.x;
  const int xcd = b & 7, idx = b >> 3;
  const int rb = xcd * 4 + (idx & 3);
  const int ct = idx >> 2;
  const int row0 = rb * 64;
  const int g = lane >> 4, q = (lane & 15) >> 2, p = lane & 3;
  const int arow = wm * 16 + (lane & 15);

  f32x4 acc[3] = {}, st = {}, cs[3] = {};
  float* scr = (float*)(smem + 142 * 1024);

  STAMP(ws, 0, 0);
  if (ct < 7) {
    gemm64<10, K1_RING, true>(smem, x + (size_t)row0 * D_, D_, wq + ct * 96, D_, ct, wave, lane, acc, st, cs);
    STAMP(ws, 0, 1);
  } else {
    constexpr int WOFF = 80 * 1024;
    {
      const unsigned vo = (unsigned)((lane >> 3) * D_ + (((lane & 7) ^ (lane >> 3)) << 3)) * 2u;
      const char* ab = (const char*)(x + (size_t)(row0 + wave * 8) * D_);
      const unsigned l0 = lds_addr(smem);
#pragma unroll
      for (int s = 0; s < 10; ++s)
        glds16s(vo, ab + s * 128, l0 + s * 8192 + wave * 1024);
    }
#pragma unroll
    for (int it = 0; it < 13; ++it) {
      int e = it * 512 + tid;
      if (e < 6400) {
        int k = e / 10, c4 = e - k * 10;
        const u16* src = (c4 < 5) ? (wk + k * 20 + c4 * 4) : (wv + k * 20 + (c4 - 5) * 4);
        u32x2 v = *(const u32x2*)src;
        int kb = k >> 3, cb = c4 >> 2;
        int rp = (k & 7) ^ ((kb & 1) << 2);
        *(LDS_AS u32x2*)(smem + WOFF + (kb * 3 + cb) * 256 + rp * 32 + (c4 & 3) * 8) = v;
      }
    }
    wait_vm<0>();
    __syncthreads();
    if (wn == 0 || true) {
      const int boff = g * 3 * 256 + q * 32 + p * 8;
      const int b0 = WOFF + boff + (g & 1) * 128, b1 = WOFF + boff + (1 - (g & 1)) * 128;
#pragma unroll
      for (int kstep = 0; kstep < 20; ++kstep) {
        int s = kstep >> 1, ks = kstep & 1;
        int a_off = s * 8192 + arow * 128 + (((ks * 4 + g) ^ (arow & 7)) << 4);
        bf16x8 a = lds_b128(smem + a_off);
        if (wn == 0) {
#pragma unroll
          for (int j = 0; j < 3; ++j) {
            bf16x8 w = lds_tr_pair(smem + b0 + kstep * 4 * 3 * 256 + j * 256, smem + b1 + kstep * 4 * 3 * 256 + j * 256);
            acc[j] = mfma16(w, a, acc[j]);
          }
          st = mfma16(ones8(), a, st);
        } else {
          st = mfma16(a, a, st);
          if ((kstep & 3) == wm) {
#pragma unroll
            for (int j = 0; j < 3; ++j) {
              bf16x8 w = lds_tr_pair(smem + b0 + kstep * 4 * 3 * 256 + j * 256, smem + b1 + kstep * 4 * 3 * 256 + j * 256);
              cs[j] = mfma16(w, ones8(), cs[j]);
            }
          }
        }
      }
    }
  }
  if (wn == 0) {
    if (g == 0) scr[wm * 16 + lane] = st[0];
  } else {
    if (g == q) scr[64 + wm * 16 + (lane & 15)] = st[p == 0 ? 0 : p == 1 ? 1 : p == 2 ? 2 : 3];
  }
  if ((lane & 15) == 0) {
    if (ct < 7) {
#pragma unroll
      for (int j = 0; j < 3; ++j) *(LDS_AS f32x4*)(&scr[128 + wm * 96 + acc_col(ct, wn, j) + g * 4]) = cs[j];
    } else if (wn == 1) {
#pragma unroll
      for (int j = 0; j < 3; ++j) *(LDS_AS f32x4*)(&scr[128 + wm * 96 + j * 16 + g * 4]) = cs[j];
    }
  }
  __syncthreads();
  const float rsum = scr[arow], rsq = scr[64 + arow];
  const float mean = rsum * (1.0f / D_);
  const float var = fmaxf(rsq * (1.0f / D_) - mean * mean, 0.0f);
  const float rstd = rsqrtf(var + LN_EPS);
  const int row = row0 + arow;
  if (ct < 7) {
    u16* Q = (u16*)(ws + WS_Q);
    const int n0 = ct * 96;
#pragma unroll
    for (int j = 0; j < 3; ++j) {
      if (ct == 6 && j == 2) continue;
      int nl = acc_col(ct, wn, j) + g * 4;
      f32x4 c = *(LDS_AS f32x4*)&scr[128 + nl];
      c += *(LDS_AS f32x4*)&scr[128 + 96 + nl];
      c += *(LDS_AS f32x4*)&scr[128 + 192 + nl];
      c += *(LDS_AS f32x4*)&scr[128 + 288 + nl];
      float v[4];
#pragma unroll
      for (int r = 0; r < 4; ++r) v[r] = (acc[j][r] - mean * c[r]) * (rstd * QSCALE);
      u32x2 o = {pk_bf16(v[0], v[1]), pk_bf16(v[2], v[3])};
      *(u32x2*)(Q + (size_t)row * D_ + n0 + nl) = o;
    }
  } else if (wn == 0) {
    char* kv = ws + WS_KV + (size_t)(row >> 5) * KVT;
    const int kk = row & 31;
    float ksq = 0.f;
#pragma unroll
    for (int j = 0; j < 3; ++j) {
      int nl = j * 16 + g * 4;
      if (nl >= 40) continue;
      f32x4 c = *(LDS_AS f32x4*)&scr[128 + nl];
      c += *(LDS_AS f32x4*)&scr[128 + 96 + nl];
      c += *(LDS_AS f32x4*)&scr[128 + 192 + nl];
      c += *(LDS_AS f32x4*)&scr[128 + 288 + nl];
      float v[4];
#pragma unroll
      for (int r = 0; r < 4; ++r) v[r] = (acc[j][r] - mean * c[r]) * rstd;
      u32x2 o = {pk_bf16(v[0], v[1]), pk_bf16(v[2], v[3])};
      if (nl < 20) {
        float a0 = bf_lo(o[0]), a1 = bf_hi(o[0]), a2 = bf_lo(o[1]), a3 = bf_hi(o[1]);
        ksq += a0 * a0 + a1 * a1 + a2 * a2 + a3 * a3;
        if (nl < 16) {
          *(u32x2*)(kv + ((nl >> 3) * 32 + kk) * 16 + (nl & 7) * 2) = o;
        } else {
          *(u32x2*)(kv + 1024 + kk * 8) = o;
          u32x2 cst = {0x3f803f80u, 0x00003f80u};
          *(u32x2*)(kv + 1024 + 256 + kk * 8) = cst;
        }
      } else {
        int d = nl - 20;
        int s = kk >> 4, k16 = kk & 15;
        int h = (k16 >> 2) & 1, jj = ((k16 >> 3) << 2) | (k16 & 3);
        u16* vt = (u16*)(kv + 1536) + ((s * 2 + h) * 21 + d) * 8 + jj;
        vt[0] = (u16)(o[0] & 0xffff);
        vt[8] = (u16)(o[0] >> 16);
        vt[16] = (u16)(o[1] & 0xffff);
        vt[24] = (u16)(o[1] >> 16);
        if (d == 16) vt[32] = (u16)0x3f80;
      }
    }
    ksq += __shfl_xor(ksq, 16);
    ksq += __shfl_xor(ksq, 32);
#pragma unroll
    for (int o = 1; o < 16; o <<= 1) ksq = fmaxf(ksq, __shfl_xor(ksq, o));
    if (lane == 0) ((float*)(ws + WS_KMAX))[rb * 4 + wm] = ksq;
  }
  STAMP(ws, 0, 2);
}

constexpr int AT_STAGE = 8 * KVT;
constexpr int AT_LDS = 4 * AT_STAGE;

__device__ __forceinline__ void split3(float m, unsigned& w0, unsigned& w1) {
  float a = -m;
  unsigned ua = __builtin_bit_cast(unsigned, a) & 0xffff0000u;
  float r1 = a - __builtin_bit_cast(float, ua);
  unsigned ub = __builtin_bit_cast(unsigned, r1) & 0xffff0000u;
  float r2 = r1 - __builtin_bit_cast(float, ub);
  unsigned uc = __builtin_bit_cast(unsigned, r2) & 0xffff0000u;
  w0 = (ua >> 16) | ub;
  w1 = uc >> 16;
}

__device__ __forceinline__ void attn_tile(const char* kt, int lane, int voff, const bf16x8 (&q0)[2], const s16x4 (&q1)[2],
                                          f32x16 (&o)[2]) {
  bf16x8 k0 = lds_b128(kt + lane * 16);
  s16x4 k1 = *(const LDS_AS s16x4*)(kt + 1024 + lane * 8);
  bf16x8 v0 = lds_b128(kt + 1536 + voff);
  bf16x8 v1 = lds_b128(kt + 1536 + 672 + voff);
#pragma unroll
  for (int t = 0; t < 2; ++t) {
    f32x16 z = {};
    f32x16 s = __builtin_amdgcn_mfma_f32_32x32x16_bf16(k0, q0[t], z, 0, 0, 0);
    s = __builtin_amdgcn_mfma_f32_32x32x8bf16_1k(k1, q1[t], s, 0, 0, 0);
    u32x4 p0, p1;
#pragma unroll
    for (int i = 0; i < 4; ++i) {
      p0[i] = pk_bf16(__builtin_amdgcn_exp2f(s[2 * i]), __builtin_amdgcn_exp2f(s[2 * i + 1]));
      p1[i] = pk_bf16(__builtin_amdgcn_exp2f(s[8 + 2 * i]), __builtin_amdgcn_exp2f(s[9 + 2 * i]));
    }
    o[t] = __builtin_amdgcn_mfma_f32_32x32x16_bf16(v0, __builtin_bit_cast(bf16x8, p0), o[t], 0, 0, 0);
    o[t] = __builtin_amdgcn_mfma_f32_32x32x16_bf16(v1, __builtin_bit_cast(bf16x8, p1), o[t], 0, 0, 0);
  }
}

__global__ __launch_bounds__(512, 2) void k2_attn(const u16* __restrict__ wg, const u16* __restrict__ wu,
                                                  char* __restrict__ ws) {
  extern __shared__ __attribute__((aligned(16))) char smem[];
  const int tid = threadIdx.x, lane = tid & 63;
  const int wave = __builtin_amdgcn_readfirstlane(tid >> 6);
  const int grp = wave >> 2, wq = wave & 3;
  const int r = lane & 31, h = lane >> 5;
  const char* kvg = ws + WS_KV;
  const int pos0 = blockIdx.x * 8 + wq * 2;

  unsigned kvo[6];
#pragma unroll
  for (int it = 0; it < 6; ++it) {
    int c = it * 512 + wave * 64 + lane;
    int gg = c >= 1440;
    kvo[it] = (unsigned)(gg * 4 * AT_STAGE + (c - gg * 1440) * 16);
  }
  const unsigned l0 = lds_addr(smem);
  auto issue = [&](int st, int buf) {
#pragma unroll
    for (int it = 0; it < 6; ++it) {
      if (it < 5 || wave < 5) glds16s(kvo[it], kvg + (size_t)st * AT_STAGE, l0 + buf * 2 * AT_STAGE + (it * 512 + wave * 64) * 16);
    }
  };
  STAMP(ws, 1, 0);
  issue(0, 0);

  {
    const int c0 = blockIdx.x * 12;
    const u16* w = (c0 < F_) ? (wg + c0) : (wu + (c0 - F_));
    float a[12];
#pragma unroll
    for (int i = 0; i < 12; ++i) a[i] = 0.f;
#pragma unroll
    for (int it = 0; it < 2; ++it) {
      int k = tid + it * 512;
      if (k < D_) {
        const u32x2* s = (const u32x2*)(w + (size_t)k * F_);
        u32x2 v0 = s[0], v1 = s[1], v2 = s[2];
        a[0] += bf_lo(v0[0]); a[1] += bf_hi(v0[0]); a[2] += bf_lo(v0[1]); a[3] += bf_hi(v0[1]);
        a[4] += bf_lo(v1[0]); a[5] += bf_hi(v1[0]); a[6] += bf_lo(v1[1]); a[7] += bf_hi(v1[1]);
        a[8] += bf_lo(v2[0]); a[9] += bf_hi(v2[0]); a[10] += bf_lo(v2[1]); a[11] += bf_hi(v2[1]);
      }
    }
#pragma unroll
    for (int i = 0; i < 12; ++i) {
#pragma unroll
      for (int o = 1; o < 64; o <<= 1) a[i] += __shfl_xor(a[i], o);
    }
    float* red = (float*)(smem + AT_LDS);
    if (lane == 0) {
#pragma unroll
      for (int i = 0; i < 12; ++i) red[wave * 12 + i] = a[i];
    }
  }

  float kmax2;
  {
    const float* km = (const float*)(ws + WS_KMAX);
    float m = fmaxf(km[lane], km[lane + 64]);
#pragma unroll
    for (int o = 1; o < 64; o <<= 1) m = fmaxf(m, __shfl_xor(m, o));
    kmax2 = m;
  }
  bf16x8 q0[2];
  s16x4 q1[2];
  u32x2 qtail[2];
  float mshift[2];
  const u16* Qs = (const u16*)(ws + WS_Q);
#pragma unroll
  for (int t = 0; t < 2; ++t) {
    const u16* qp = Qs + (size_t)(pos0 + t) * D_ + r * 20;
    u32x2 a = *(const u32x2*)(qp + 8 * h), b2 = *(const u32x2*)(qp + 8 * h + 4);
    u32x4 qq = {a[0], a[1], b2[0], b2[1]};
    q0[t] = __builtin_bit_cast(bf16x8, qq);
    u32x2 tl = *(const u32x2*)(qp + 16);
    qtail[t] = tl;
    float ss = 0.f;
#pragma unroll
    for (int i = 0; i < 4; ++i) { float x0 = bf_lo(qq[i]), x1 = bf_hi(qq[i]); ss += x0 * x0 + x1 * x1; }
    if (h == 0) {
#pragma unroll
      for (int i = 0; i < 2; ++i) { float x0 = bf_lo(tl[i]), x1 = bf_hi(tl[i]); ss += x0 * x0 + x1 * x1; }
    }
    ss += __shfl_xor(ss, 32);
    mshift[t] = sqrtf(ss * kmax2) * 1.0001f;
  }

  const int voff = (h * 21 + (r < 21 ? r : 20)) * 16;
  f32x16 o[2];
  float* xch = (float*)smem;
  int* flag = (int*)(smem + AT_LDS + 512);

  for (int attempt = 0;; ++attempt) {
#pragma unroll
    for (int t = 0; t < 2; ++t) {
      unsigned w0, w1;
      split3(mshift[t], w0, w1);
      u32x2 f = (h == 0) ? qtail[t] : u32x2{w0, w1};
      q1[t] = __builtin_bit_cast(s16x4, f);
#pragma unroll
      for (int i = 0; i < 16; ++i) o[t][i] = 0.f;
    }
    if (attempt) issue(0, 0);
    STAMP(ws, 1, 1);
#pragma unroll 1
    for (int st = 0; st < 4; ++st) {
      wait_vm<0>();
      bar();
      if (st < 3) issue(st + 1, (st + 1) & 1);
      const char* kb = smem + (st & 1) * 2 * AT_STAGE + grp * AT_STAGE;
#pragma unroll
      for (int kt = 0; kt < 8; ++kt) attn_tile(kb + kt * KVT, lane, voff, q0, q1, o);
    }
    __syncthreads();
    STAMP(ws, 1, 2);
    if (grp == 1) {
#pragma unroll
      for (int t = 0; t < 2; ++t)
#pragma unroll
        for (int i = 0; i < 12; ++i) xch[((wq * 2 + t) * 12 + i) * 64 + lane] = o[t][i];
    }
    if (tid == 0) *flag = 0;
    __syncthreads();
    bool bad = false;
    float linv[2];
    if (grp == 0) {
#pragma unroll
      for (int t = 0; t < 2; ++t) {
#pragma unroll
        for (int i = 0; i < 12; ++i) o[t][i] += xch[((wq * 2 + t) * 12 + i) * 64 + lane];
        float l = __shfl(o[t][8], 32 + r);
        bad |= !(l >= 5.421010862427522e-20f);
        linv[t] = 1.0f / l;
      }
      if (attempt == 0 && bad) *flag = 1;
    }
    __syncthreads();
    if (attempt == 1 || *flag == 0) {
      if (grp == 0) {
        u16* O = (u16*)(ws + WS_O);
#pragma unroll
        for (int t = 0; t < 2; ++t) {
          u16* op = O + (size_t)(pos0 + t) * D_ + r * 20 + 4 * h;
#pragma unroll
          for (int gq = 0; gq < 3; ++gq) {
            if (gq == 2 && h == 1) continue;
            u32x2 v = {pk_bf16(o[t][4 * gq] * linv[t], o[t][4 * gq + 1] * linv[t]),
                       pk_bf16(o[t][4 * gq + 2] * linv[t], o[t][4 * gq + 3] * linv[t])};
            *(u32x2*)(op + 8 * gq) = v;
          }
        }
      }
      break;
    }
    __syncthreads();
#pragma unroll 1
    for (int t = 0; t < 2; ++t) {
      const u16* qp = Qs + (size_t)(pos0 + t) * D_ + r * 20;
      float qf[20];
#pragma unroll
      for (int i = 0; i < 10; ++i) { unsigned u = *(const unsigned*)(qp + 2 * i); qf[2 * i] = bf_lo(u); qf[2 * i + 1] = bf_hi(u); }
      float mx = -INFINITY;
#pragma unroll 1
      for (int kk = 0; kk < 512; ++kk) {
        int key = grp * 1024 + h * 512 + kk;
        const char* t0 = kvg + (size_t)(key >> 5) * KVT;
        int kr = key & 31;
        u32x4 a = *(const u32x4*)(t0 + kr * 16), b2 = *(const u32x4*)(t0 + 512 + kr * 16);
        u32x2 c = *(const u32x2*)(t0 + 1024 + kr * 8);
        float sacc = 0.f;
#pragma unroll
        for (int i = 0; i < 4; ++i) {
          sacc += qf[2 * i] * bf_lo(a[i]) + qf[2 * i + 1] * bf_hi(a[i]);
          sacc += qf[8 + 2 * i] * bf_lo(b2[i]) + qf[9 + 2 * i] * bf_hi(b2[i]);
        }
        sacc += qf[16] * bf_lo(c[0]) + qf[17] * bf_hi(c[0]) + qf[18] * bf_lo(c[1]) + qf[19] * bf_hi(c[1]);
        mx = fmaxf(mx, sacc);
      }
      mx = fmaxf(mx, __shfl_xor(mx, 32));
      if (h == 0) xch[(grp * 8 + wq * 2 + t) * 32 + r] = mx;
    }
    __syncthreads();
#pragma unroll
    for (int t = 0; t < 2; ++t) mshift[t] = fmaxf(xch[(wq * 2 + t) * 32 + r], xch[(8 + wq * 2 + t) * 32 + r]);
    __syncthreads();
  }
  if (tid < 12) {
    const float* red = (const float*)(smem + AT_LDS);
    float s = 0.f;
#pragma unroll
    for (int w = 0; w < 8; ++w) s += red[w * 12 + tid];
    ((float*)(ws + WS_CS))[blockIdx.x * 12 + tid] = s;
  }
  STAMP(ws, 1, 3);
}

constexpr int G2_RING = 7;
constexpr int G2_LDS = G2_RING * ST64;

template <int K, bool STATS>
__global__ __launch_bounds__(512, 2) void k_gemm96(const u16* __restrict__ A, const u16* __restrict__ W,
                                                   const u16* __restrict__ res, u16* __restrict__ out,
                                                   float* __restrict__ stats, char* __restrict__ ws) {
  extern __shared__ __attribute__((aligned(16))) char smem[];
  const int tid = threadIdx.x, lane = tid & 63;
  const int wave = __builtin_amdgcn_readfirstlane(tid >> 6);
  const int wm = wave & 3, wn = wave >> 2;
  const int b = blockIdx.x;
  const int xcd = b & 7, idx = b >> 3;
  const int rb = xcd * 4 + (idx & 3), ct = idx >> 2;
  const int row0 = rb * 64, n0 = ct * 96;
  const int g = lane >> 4;
  const int arow = wm * 16 + (lane & 15);
  const int row = row0 + arow;
  const int nj = (ct == 6) ? 2 : 3;
  STAMP(ws, STATS ? 2 : 4, 0);

  u32x2 rv[3];
#pragma unroll
  for (int j = 0; j < 3; ++j) {
    int c = (j < nj) ? acc_col(ct, wn, j) : 0;
    rv[j] = *(const u32x2*)(res + (size_t)row * D_ + n0 + c + g * 4);
  }
  f32x4 acc[3] = {}, st = {}, cs[3] = {};
  gemm64<K / 64, G2_RING, false>(smem, A + (size_t)row0 * K, K, W + n0, D_, ct, wave, lane, acc, st, cs);
  STAMP(ws, STATS ? 2 : 4, 1);

  float sum = 0.f;
  float hv[3][4];
#pragma unroll
  for (int j = 0; j < 3; ++j) {
    if (j < nj) {
      float v0 = acc[j][0] + bf_lo(rv[j][0]), v1 = acc[j][1] + bf_hi(rv[j][0]);
      float v2 = acc[j][2] + bf_lo(rv[j][1]), v3 = acc[j][3] + bf_hi(rv[j][1]);
      u32x2 o = {pk_bf16(v0, v1), pk_bf16(v2, v3)};
      *(u32x2*)(out + (size_t)row * D_ + n0 + acc_col(ct, wn, j) + g * 4) = o;
      if (STATS) {
        hv[j][0] = bf_lo(o[0]); hv[j][1] = bf_hi(o[0]); hv[j][2] = bf_lo(o[1]); hv[j][3] = bf_hi(o[1]);
        sum += hv[j][0] + hv[j][1] + hv[j][2] + hv[j][3];
      }
    } else if (STATS) {
      hv[j][0] = hv[j][1] = hv[j][2] = hv[j][3] = 0.f;
    }
  }
  if (STATS) {
    float* scr = (float*)smem;
    sum += __shfl_xor(sum, 16);
    sum += __shfl_xor(sum, 32);
    __syncthreads();
    if (g == 0) scr[wn * 64 + arow] = sum;
    __syncthreads();
    const float tot = scr[arow] + scr[64 + arow];
    const float mu = tot * ((ct == 6) ? (1.0f / 64.0f) : (1.0f / 96.0f));
    float m2 = 0.f;
#pragma unroll
    for (int j = 0; j < 3; ++j) {
      if (j < nj) {
#pragma unroll
        for (int r = 0; r < 4; ++r) { float d = hv[j][r] - mu; m2 += d * d; }
      }
    }
    m2 += __shfl_xor(m2, 16);
    m2 += __shfl_xor(m2, 32);
    if (g == 0) scr[128 + wn * 64 + arow] = m2;
    __syncthreads();
    if (wn == 0 && g == 0) {
      f32x2 o = {tot, scr[128 + arow] + scr[192 + arow]};
      *(f32x2*)(stats + ((size_t)row * 8 + ct) * 2) = o;
    }
  }
  STAMP(ws, STATS ? 2 : 4, 2);
}

constexpr int G3_RING = 3;
constexpr int G3_LDS = G3_RING * 40 * 1024 + 2048;

__global__ __launch_bounds__(512, 2) void k4_ffn_up(const u16* __restrict__ Hh, const u16* __restrict__ wg,
                                                    const u16* __restrict__ wu, const float* __restrict__ stats,
                                                    const float* __restrict__ cs, u16* __restrict__ act,
                                                    char* __restrict__ ws) {
  extern __shared__ __attribute__((aligned(16))) char smem[];
  constexpr int ST = 40 * 1024;
  const int tid = threadIdx.x, lane = tid & 63;
  const int wave = __builtin_amdgcn_readfirstlane(tid >> 6);
  const int wm = wave & 3, wn = wave >> 2;
  const int b = blockIdx.x;
  const int xcd = b & 7, idx = b >> 3;
  const int rb = (xcd & 1) * 8 + (idx & 7);
  const int ct = (xcd >> 1) * 4 + (idx >> 3);
  const int row0 = rb * 128, n0 = ct * 96;
  const int g = lane >> 4;
  STAMP(ws, 3, 0);

  const int c64 = (ct & 1) ? 32 : 0, c32 = (ct & 1) ? 0 : 64;
  const unsigned va = voff_a(wave * 16, lane, D_);
  const unsigned v64 = voff_g64(wave, lane, F_, c64), v32 = voff_g32(wave & 3, lane, F_, c32);
  const char* abase = (const char*)(Hh + (size_t)row0 * D_);
  const char* gbase = (const char*)(wg + n0);
  const char* ubase = (const char*)(wu + n0);
  const unsigned l0 = lds_addr(smem);
  auto issue = [&](int s, int slot) {
    const unsigned d = l0 + slot * ST;
    const char* gw = gbase + (size_t)s * 64 * F_ * 2;
    const char* uw = ubase + (size_t)s * 64 * F_ * 2;
    glds16s(va, abase + s * 128, d + wave * 2048);
    glds16s(va, abase + s * 128 + 8 * D_ * 2, d + wave * 2048 + 1024);
    glds16s(v64, gw, d + 16384 + wave * 1024);
    glds16s(v64, uw, d + 24576 + wave * 1024);
    glds16s(v32, wave < 4 ? gw : uw, d + 32768 + wave * 1024);
  };
#pragma unroll
  for (int s = 0; s < G3_RING - 1; ++s) issue(s, s);

  float* scr = (float*)(smem + G3_RING * ST);
  float mean_r = 0.f, rstd_r = 0.f;
  if (tid < 128) {
    const f32x4* sp = (const f32x4*)(stats + (size_t)(row0 + tid) * 16);
    f32x4 s0 = sp[0], s1 = sp[1], s2 = sp[2];
    f32x2 s3 = *(const f32x2*)(sp + 3);
    float tot = s0[0] + s0[2] + s1[0] + s1[2] + s2[0] + s2[2] + s3[0];
    float mean = tot * (1.0f / D_);
    float m2 = s0[1] + s0[3] + s1[1] + s1[3] + s2[1] + s2[3] + s3[1];
    float c = 0.f, d;
    d = s0[0] * (1.f / 96.f) - mean; c += d * d; d = s0[2] * (1.f / 96.f) - mean; c += d * d;
    d = s1[0] * (1.f / 96.f) - mean; c += d * d; d = s1[2] * (1.f / 96.f) - mean; c += d * d;
    d = s2[0] * (1.f / 96.f) - mean; c += d * d; d = s2[2] * (1.f / 96.f) - mean; c += d * d;
    d = s3[0] * (1.f / 64.f) - mean;
    float var = (m2 + 96.f * c + 64.f * d * d) * (1.0f / D_);
    mean_r = mean;
    rstd_r = rsqrtf(var + LN_EPS);
  }

  const int wo0 = 16384 + roff_g64(wn * 2, lane), wo1 = 16384 + roff_g64(wn * 2 + 1, lane), wo2 = 32768 + roff_g32(wn, lane);
  const int ar0 = wm * 32 + (lane & 15);
  f32x4 ag[2][3] = {}, au[2][3] = {};
#pragma unroll
  for (int s = 0; s < 10; ++s) {
    if (s < 9) wait_vm<5>();
    else wait_vm<0>();
    bar();
    if (s + 2 < 10) issue(s + 2, (s + 2) % 3);
    const char* buf = smem + (s % 3) * ST;
#pragma unroll
    for (int ks = 0; ks < 2; ++ks) {
      bf16x8 a[2];
#pragma unroll
      for (int mi = 0; mi < 2; ++mi) {
        int ar = ar0 + mi * 16;
        a[mi] = lds_b128(buf + ar * 128 + (((ks * 4 + g) ^ (ar & 7)) << 4));
      }
#pragma unroll
      for (int j = 0; j < 3; ++j) {
        bf16x8 w_g = j == 0 ? rd_g64(buf + wo0, ks) : j == 1 ? rd_g64(buf + wo1, ks) : rd_g32(buf + wo2, ks);
        bf16x8 w_u = j == 0 ? rd_g64(buf + wo0 + 8192, ks) : j == 1 ? rd_g64(buf + wo1 + 8192, ks) : rd_g32(buf + wo2 + 4096, ks);
#pragma unroll
        for (int mi = 0; mi < 2; ++mi) {
          ag[mi][j] = mfma16(w_g, a[mi], ag[mi][j]);
          au[mi][j] = mfma16(w_u, a[mi], au[mi][j]);
        }
      }
    }
  }
  STAMP(ws, 3, 1);
  if (tid < 128) { scr[tid * 2] = mean_r; scr[tid * 2 + 1] = rstd_r; }
  __syncthreads();
#pragma unroll
  for (int mi = 0; mi < 2; ++mi) {
    const int ar = ar0 + mi * 16;
    const f32x2 ms = *(LDS_AS f32x2*)&scr[ar * 2];
    const float rstd = ms[1], nm = -ms[0] * ms[1];
#pragma unroll
    for (int j = 0; j < 3; ++j) {
      const int n = n0 + acc_col(ct, wn, j) + g * 4;
      const f32x4 cg = *(const f32x4*)(cs + n), cu = *(const f32x4*)(cs + F_ + n);
      float o[4];
#pragma unroll
      for (int r = 0; r < 4; ++r) {
        float gv = ag[mi][j][r] * rstd + nm * cg[r];
        float uv = au[mi][j][r] * rstd + nm * cu[r];
        float t = gv * gv * (0.044715f * 1.5957691216057308f * 1.4426950408889634f) + (1.5957691216057308f * 1.4426950408889634f);
        float e = __builtin_amdgcn_exp2f(-gv * t);
        o[r] = gv * uv * __builtin_amdgcn_rcpf(1.0f + e);
      }
      u32x2 v = {pk_bf16(o[0], o[1]), pk_bf16(o[2], o[3])};
      *(u32x2*)(act + (size_t)(row0 + ar) * F_ + n) = v;
    }
  }
  STAMP(ws, 3, 2);
}

extern "C" void kernel_launch(void* const* d_in, const int* in_sizes, int n_in, void* d_out, int out_size,
                              void* d_ws, size_t ws_size, hipStream_t stream) {
  const u16* x = (const u16*)d_in[0];
  const u16* wq = (const u16*)d_in[1];
  const u16* wk = (const u16*)d_in[2];
  const u16* wv = (const u16*)d_in[3];
  const u16* wo = (const u16*)d_in[4];
  const u16* wg = (const u16*)d_in[6];
  const u16* wu = (const u16*)d_in[7];
  const u16* wd = (const u16*)d_in[8];
  char* ws = (char*)d_ws;
  u16* out = (u16*)d_out;

  (void)hipFuncSetAttribute((const void*)k1_qkv, hipFuncAttributeMaxDynamicSharedMemorySize, K1_LDS);
  (void)hipFuncSetAttribute((const void*)k2_attn, hipFuncAttributeMaxDynamicSharedMemorySize, AT_LDS + 1024);
  (void)hipFuncSetAttribute((const void*)k_gemm96<640, true>, hipFuncAttributeMaxDynamicSharedMemorySize, G2_LDS);
  (void)hipFuncSetAttribute((const void*)k_gemm96<1536, false>, hipFuncAttributeMaxDynamicSharedMemorySize, G2_LDS);
  (void)hipFuncSetAttribute((const void*)k4_ffn_up, hipFuncAttributeMaxDynamicSharedMemorySize, G3_LDS);

  k1_qkv<<<256, 512, K1_LDS, stream>>>(x, wq, wk, wv, ws);
  k2_attn<<<256, 512, AT_LDS + 1024, stream>>>(wg, wu, ws);
  k_gemm96<640, true><<<224, 512, G2_LDS, stream>>>((const u16*)(ws + WS_O), wo, x, (u16*)(ws + WS_H),
                                                    (float*)(ws + WS_STAT), ws);
  k4_ffn_up<<<256, 512, G3_LDS, stream>>>((const u16*)(ws + WS_H), wg, wu, (const float*)(ws + WS_STAT),
                                          (const float*)(ws + WS_CS), (u16*)(ws + WS_ACT), ws);
  k_gemm96<1536, false><<<224, 512, G2_LDS, stream>>>((const u16*)(ws + WS_ACT), wd, (const u16*)(ws + WS_H), out,
                                                      (float*)(ws + WS_STAT), ws);
}
```

```cpp
#include <hip/hip_runtime.h>
#include <hip/hip_bf16.h>

typedef __attribute__((ext_vector_type(8))) __bf16 bf16x8;
typedef __attribute__((ext_vector_type(4))) __bf16 bf16x4;
typedef __attribute__((ext_vector_type(2))) __bf16 bf16x2;
typedef __attribute__((ext_vector_type(4))) short s16x4;
typedef __attribute__((ext_vector_type(16))) float f32x16;
typedef __attribute__((ext_vector_type(4))) float f32x4;
typedef __attribute__((ext_vector_type(2))) float f32x2;
typedef __attribute__((ext_vector_type(4))) unsigned u32x4;
typedef __attribute__((ext_vector_type(2))) unsigned u32x2;
typedef unsigned short u16;

#define LDS_AS __attribute__((address_space(3)))
#define GLB_AS __attribute__((address_space(1)))

#define PF 0
constexpr int S_ = 2048, D_ = 640, F_ = 1536;
constexpr float LN_EPS = 1e-5f;
constexpr float QSCALE = 0.22360679774997896f * 1.4426950408889634f;

constexpr int KVT = 2880;
constexpr int KV_BYTES = 64 * KVT;

constexpr size_t WS_Q = 0;
constexpr size_t WS_KV = WS_Q + (size_t)S_ * D_ * 2;
constexpr size_t WS_KMAX = WS_KV + KV_BYTES;
constexpr size_t WS_O = WS_KMAX + 512;
constexpr size_t WS_H = WS_O + (size_t)S_ * D_ * 2;
constexpr size_t WS_STAT = WS_H + (size_t)S_ * D_ * 2;
constexpr size_t WS_CS = WS_STAT + (size_t)S_ * 8 * 2 * 4;
constexpr size_t WS_ACT = WS_CS + 3072 * 4;

__device__ __forceinline__ unsigned pk_bf16(float a, float b) {
  f32x2 f = {a, b};
  return __builtin_bit_cast(unsigned, __builtin_convertvector(f, bf16x2));
}
__device__ __forceinline__ float bf_lo(unsigned u) { return __builtin_bit_cast(float, u << 16); }
__device__ __forceinline__ float bf_hi(unsigned u) { return __builtin_bit_cast(float, u & 0xffff0000u); }

#define STAMP(ws_, kid, i)
#define STAMP2(kid, i)
template <int N> __device__ __forceinline__ void wait_vm() { asm volatile("s_waitcnt vmcnt(%0)" ::"i"(N) : "memory"); }
__device__ __forceinline__ void bar() { __builtin_amdgcn_s_barrier(); }
__device__ __forceinline__ void glds16s(unsigned voff, const void* sbase, unsigned lds) {
  asm volatile("s_mov_b32 m0, %2\n\ts_nop 3\n\tglobal_load_lds_dwordx4 %0, %1" ::"v"(voff), "s"(sbase), "s"(lds)
               : "memory");
}
__device__ __forceinline__ void pf_line(const char* addr, unsigned lds_dump) {
  asm volatile("s_mov_b32 m0, %1\n\ts_nop 3\n\tglobal_load_lds_dword %0, off" ::"v"(addr), "s"(lds_dump) : "memory");
}
template <int NI>
__device__ __forceinline__ void pf_regions(const char* A, int nA, const char* B, int nB, int part, int nparts, int wave,
                                           int lane, unsigned lds_dump) {
  const int per = (nA + nB + nparts - 1) / nparts;
#pragma unroll
  for (int t = 0; t < NI; ++t) {
    int i0 = (wave + t * 8) * 64;
    if (i0 < per) {
      int l = part * per + i0 + lane;
      l = min(l, nA + nB - 1);
      pf_line(l < nA ? A + (size_t)l * 128 : B + (size_t)(l - nA) * 128, lds_dump);
    }
  }
}
__device__ __forceinline__ unsigned lds_addr(const char* p) { return (unsigned)(size_t)(const LDS_AS char*)p; }
__device__ __forceinline__ void lds_fence() { asm volatile("s_waitcnt lgkmcnt(0)" ::: "memory"); }
__device__ __forceinline__ bf16x8 lds_b128(const char* p) { return *(const LDS_AS bf16x8*)(p); }
__device__ __forceinline__ bf16x8 lds_tr_pair(const char* p0, const char* p1) {
  bf16x4 a = __builtin_amdgcn_ds_read_tr16_b64_v4bf16((LDS_AS bf16x4*)p0);
  bf16x4 b = __builtin_amdgcn_ds_read_tr16_b64_v4bf16((LDS_AS bf16x4*)p1);
  return __builtin_shufflevector(a, b, 0, 1, 2, 3, 4, 5, 6, 7);
}
__device__ __forceinline__ f32x4 mfma16(bf16x8 a, bf16x8 b, f32x4 c) {
  return __builtin_amdgcn_mfma_f32_16x16x32_bf16(a, b, c, 0, 0, 0);
}
__device__ __forceinline__ bf16x8 ones8() {
  u32x4 o = {0x3f803f80u, 0x3f803f80u, 0x3f803f80u, 0x3f803f80u};
  return __builtin_bit_cast(bf16x8, o);
}

__device__ __forceinline__ unsigned voff_a(int row, int lane, int lda) {
  return (unsigned)((row + (lane >> 3)) * lda + (((lane & 7) ^ (lane >> 3)) << 3)) * 2u;
}
__device__ __forceinline__ unsigned voff_g64(int kb, int lane, int ldw, int c64) {
  int r = lane >> 3, pos = lane & 7;
  int u = (pos >> 1) ^ (((r >> 1) & 1) | ((kb & 1) << 1));
  return (unsigned)((kb * 8 + r) * ldw + c64 + u * 16 + (pos & 1) * 8) * 2u;
}
__device__ __forceinline__ unsigned voff_g32(int kbp, int lane, int ldw, int c32) {
  int kb = kbp * 2 + (lane >> 5), r = (lane >> 2) & 7, pos = lane & 3;
  int u = (pos >> 1) ^ (kb & 1);
  return (unsigned)((kb * 8 + r) * ldw + c32 + u * 16 + (pos & 1) * 8) * 2u;
}
__device__ __forceinline__ int roff_g64(int u, int lane) {
  int g = lane >> 4, q = (lane & 15) >> 2, p = lane & 3;
  return g * 1024 + q * 128 + ((u ^ (((q >> 1) & 1) | ((g & 1) << 1))) << 5) + p * 8;
}
__device__ __forceinline__ int roff_g32(int v, int lane) {
  int g = lane >> 4, q = (lane & 15) >> 2, p = lane & 3;
  return g * 512 + q * 64 + ((v ^ (g & 1)) << 5) + p * 8;
}
__device__ __forceinline__ bf16x8 rd_g64(const char* p, int ks) { return lds_tr_pair(p + ks * 4096, p + ks * 4096 + 512); }
__device__ __forceinline__ bf16x8 rd_g32(const char* p, int ks) { return lds_tr_pair(p + ks * 2048, p + ks * 2048 + 256); }

constexpr int ST64 = 20 * 1024;

struct Frag64 { bf16x8 a[2], w[3]; };
template <int NS, int RING, int NPW, bool HAS32, bool QKV>
__device__ __forceinline__ void gemm64_loop(char* smem, const char* abase, const char* wbase, int ldw2, unsigned va,
                                            unsigned v64, unsigned v32, int wave, int lane, f32x4 (&out)[3], f32x4 (&st)[2],
                                            f32x4 (&cs)[3]) {
  const int kh = wave & 1, wm2 = (wave >> 1) & 1, wn = wave >> 2, g = lane >> 4;
  const unsigned l0 = lds_addr(smem);
  const int arow = wm2 * 32 + (lane & 15);
  const int ao = arow * 128 + (((kh * 4 + g) ^ (arow & 7)) << 4);
  const int wo0 = 8192 + kh * 4096 + roff_g64(wn * 2, lane), wo1 = 8192 + kh * 4096 + roff_g64(wn * 2 + 1, lane);
  const int wo2 = 16384 + kh * 2048 + roff_g32(wn, lane);
  f32x4 acc[2][3] = {};
  auto issue = [&](int s, int slot) {
    const unsigned d = l0 + slot * ST64 + wave * 1024;
    const char* w = wbase + (size_t)s * 64 * ldw2;
    glds16s(va, abase + s * 128, d);
    glds16s(v64, w, d + 8192);
    if (NPW == 3) glds16s(v32, w, d + 16384);
  };
  auto rd = [&](Frag64& f, int slot) {
    const char* buf = smem + slot * ST64;
    f.a[0] = lds_b128(buf + ao);
    f.w[0] = rd_g64(buf + wo0, 0);
    f.a[1] = lds_b128(buf + ao + 2048);
    f.w[1] = rd_g64(buf + wo1, 0);
    if (HAS32) f.w[2] = rd_g32(buf + wo2, 0);
  };
  auto mm = [&](const Frag64& f, int s) {
#pragma unroll
    for (int j = 0; j < (HAS32 ? 3 : 2); ++j) {
      acc[0][j] = mfma16(f.w[j], f.a[0], acc[0][j]);
      acc[1][j] = mfma16(f.w[j], f.a[1], acc[1][j]);
    }
    if (QKV) {
#pragma unroll
      for (int mi = 0; mi < 2; ++mi) {
        if (wn == 0) st[mi] = mfma16(ones8(), f.a[mi], st[mi]);
        else st[mi] = mfma16(f.a[mi], f.a[mi], st[mi]);
      }
      if ((s & 1) == wm2) {
#pragma unroll
        for (int j = 0; j < (HAS32 ? 3 : 2); ++j) cs[j] = mfma16(f.w[j], ones8(), cs[j]);
      }
    }
  };
  constexpr int NMAIN = NS - RING + 1;
  static_assert(NMAIN % 2 == 0 && RING == 7, "");
#pragma unroll
  for (int s = 0; s < RING - 1; ++s) issue(s, s);
  Frag64 f0, f1;
  wait_vm<NPW*(RING - 2)>();
  bar();
  STAMP2(QKV ? 0 : (NS == 10 ? 2 : 4), 0);
  rd(f0, 0);
  int slot = 0;
#define STEP(CUR, NXT, S, WAITN, ISSUE)                                   \
  {                                                                       \
    const int nslot = slot == RING - 1 ? 0 : slot + 1;                    \
    wait_vm<WAITN>();                                                     \
    bar();                                                                \
    STAMP2(QKV ? 0 : (NS == 10 ? 2 : 4), (S) + 1);                        \
    if (ISSUE) issue((S) + RING - 1, slot == 0 ? RING - 1 : slot - 1);    \
    rd(NXT, nslot);                                                       \
    __builtin_amdgcn_sched_barrier(0);                                    \
    mm(CUR, S);                                                           \
    __builtin_amdgcn_sched_barrier(0);                                    \
    slot = nslot;                                                         \
  }
#pragma unroll 1
  for (int s = 0; s < NMAIN; s += 2) {
    STEP(f0, f1, s, NPW*(RING - 3), true)
    STEP(f1, f0, s + 1, NPW*(RING - 3), true)
  }
  STEP(f0, f1, NS - 6, NPW * 4, false)
  STEP(f1, f0, NS - 5, NPW * 3, false)
  STEP(f0, f1, NS - 4, NPW * 2, false)
  STEP(f1, f0, NS - 3, NPW * 1, false)
  STEP(f0, f1, NS - 2, 0, false)
  mm(f1, NS - 1);
#undef STEP
  bar();
  f32x4* xs = (f32x4*)smem;
#pragma unroll
  for (int j = 0; j < 3; ++j) {
    f32x4 t0 = acc[0][j], t1 = acc[1][j];
    acc[0][j] = kh ? t1 : t0;
    acc[1][j] = kh ? t0 : t1;
    if (HAS32 || j < 2) *(LDS_AS f32x4*)&xs[(wave * 3 + j) * 64 + lane] = acc[1][j];
  }
  lds_fence();
  bar();
#pragma unroll
  for (int j = 0; j < 3; ++j) {
    if (HAS32 || j < 2) out[j] = acc[0][j] + *(LDS_AS f32x4*)&xs[((wave ^ 1) * 3 + j) * 64 + lane];
    else out[j] = acc[0][j];
  }
}

template <int NS, int RING, bool QKV>
__device__ __forceinline__ void gemm64(char* smem, const u16* A, int lda, const u16* W, int ldw, int ct, int wave, int lane,
                                       f32x4 (&acc)[3], f32x4 (&st)[2], f32x4 (&cs)[3]) {
  const int c64 = (ct & 1) ? 32 : 0, c32 = (ct & 1) ? 0 : 64;
  const unsigned va = voff_a(wave * 8, lane, lda);
  const unsigned v64 = voff_g64(wave, lane, ldw, c64), v32 = voff_g32(wave & 3, lane, ldw, c32);
  const char* ab = (const char*)A;
  const char* wb = (const char*)W;
  if (ct == 6) gemm64_loop<NS, RING, 2, false, QKV>(smem, ab, wb, ldw * 2, va, v64, v32, wave, lane, acc, st, cs);
  else if (wave < 4) gemm64_loop<NS, RING, 3, true, QKV>(smem, ab, wb, ldw * 2, va, v64, v32, wave, lane, acc, st, cs);
  else gemm64_loop<NS, RING, 2, true, QKV>(smem, ab, wb, ldw * 2, va, v64, v32, wave, lane, acc, st, cs);
}
__device__ __forceinline__ int acc_col(int ct, int wn, int j) {
  return j < 2 ? ((ct & 1) ? 32 : 0) + (wn * 2 + j) * 16 : ((ct & 1) ? 0 : 64) + wn * 16;
}

constexpr int K1_RING = 7;
constexpr int K1_LDS = 145 * 1024;

__global__ __launch_bounds__(512, 2) void k1_qkv(const u16* __restrict__ x, const u16* __restrict__ wq,
                                                 const u16* __restrict__ wk, const u16* __restrict__ wv,
                                                 char* __restrict__ ws) {
  extern __shared__ __attribute__((aligned(16))) char smem[];
  const int tid = threadIdx.x, lane = tid & 63;
  const int wave = __builtin_amdgcn_readfirstlane(tid >> 6);
  const int wm = wave & 3, wn = wave >> 2;
  const int b = blockIdx.x;
  const int xcd = b & 7, idx = b >> 3;
  const int rb = xcd * 4 + (idx & 3);
  const int ct = idx >> 2;
  const int row0 = rb * 64;
  const int g = lane >> 4, q = (lane & 15) >> 2, p = lane & 3;
  const int arow = wm * 16 + (lane & 15);

  f32x4 acc[3] = {}, st2[2] = {}, cs[3] = {};
  f32x4& st = st2[0];
  float* scr = (float*)(smem + 142 * 1024);

  STAMP(ws, 0, 0);
  if (PF) pf_regions<1>((const char*)(x + (size_t)xcd * 256 * D_), 256 * D_ * 2 / 128, (const char*)wq, D_ * D_ * 2 / 128, idx, 32, wave,
                lane, lds_addr(smem) + 143360);
  if (ct < 7) {
    gemm64<10, K1_RING, true>(smem, x + (size_t)row0 * D_, D_, wq + ct * 96, D_, ct, wave, lane, acc, st2, cs);
    STAMP(ws, 0, 1);
  } else {
    constexpr int WOFF = 80 * 1024;
    {
      const unsigned vo = (unsigned)((lane >> 3) * D_ + (((lane & 7) ^ (lane >> 3)) << 3)) * 2u;
      const char* ab = (const char*)(x + (size_t)(row0 + wave * 8) * D_);
      const unsigned l0 = lds_addr(smem);
#pragma unroll
      for (int s = 0; s < 10; ++s)
        glds16s(vo, ab + s * 128, l0 + s * 8192 + wave * 1024);
    }
    u32x2 wv_[13];
#pragma unroll
    for (int it = 0; it < 13; ++it) {
      int e = min(it * 512 + tid, 6399);
      int k = e / 10, c4 = e - k * 10;
      const u16* src = (c4 < 5) ? (wk + k * 20 + c4 * 4) : (wv + k * 20 + (c4 - 5) * 4);
      wv_[it] = *(const u32x2*)src;
    }
#pragma unroll
    for (int it = 0; it < 13; ++it) {
      int e = min(it * 512 + tid, 6399);
      int k = e / 10, c4 = e - k * 10;
      int kb = k >> 3, cb = c4 >> 2;
      int rp = (k & 7) ^ ((kb & 1) << 2);
      *(LDS_AS u32x2*)(smem + WOFF + (kb * 3 + cb) * 256 + rp * 32 + (c4 & 3) * 8) = wv_[it];
    }
    wait_vm<0>();
    __syncthreads();
    if (wn == 0 || true) {
      const int boff = g * 3 * 256 + q * 32 + p * 8;
      const int b0 = WOFF + boff + (g & 1) * 128, b1 = WOFF + boff + (1 - (g & 1)) * 128;
#pragma unroll
      for (int kstep = 0; kstep < 20; ++kstep) {
        int s = kstep >> 1, ks = kstep & 1;
        int a_off = s * 8192 + arow * 128 + (((ks * 4 + g) ^ (arow & 7)) << 4);
        bf16x8 a = lds_b128(smem + a_off);
        if (wn == 0) {
#pragma unroll
          for (int j = 0; j < 3; ++j) {
            bf16x8 w = lds_tr_pair(smem + b0 + kstep * 4 * 3 * 256 + j * 256, smem + b1 + kstep * 4 * 3 * 256 + j * 256);
            acc[j] = mfma16(w, a, acc[j]);
          }
          st = mfma16(ones8(), a, st);
        } else {
          st = mfma16(a, a, st);
          if ((kstep & 3) == wm) {
#pragma unroll
            for (int j = 0; j < 3; ++j) {
              bf16x8 w = lds_tr_pair(smem + b0 + kstep * 4 * 3 * 256 + j * 256, smem + b1 + kstep * 4 * 3 * 256 + j * 256);
              cs[j] = mfma16(w, ones8(), cs[j]);
            }
          }
        }
      }
    }
  }
  if (ct < 7) {
    const int kh = wave & 1, r32 = (wave & 2) * 16;
#pragma unroll
    for (int mi = 0; mi < 2; ++mi) {
      if (wn == 0) {
        if (g == 0) scr[kh * 64 + r32 + mi * 16 + lane] = st2[mi][0];
      } else {
        if (g == q) scr[128 + kh * 64 + r32 + mi * 16 + (lane & 15)] = st2[mi][p == 0 ? 0 : p == 1 ? 1 : p == 2 ? 2 : 3];
      }
    }
  } else if (wn == 0) {
    if (g == 0) { scr[wm * 16 + lane] = st[0]; scr[64 + wm * 16 + lane] = 0.f; }
  } else {
    if (g == q) {
      scr[128 + wm * 16 + (lane & 15)] = st[p == 0 ? 0 : p == 1 ? 1 : p == 2 ? 2 : 3];
      scr[192 + wm * 16 + (lane & 15)] = 0.f;
    }
  }
  if ((lane & 15) == 0) {
    if (ct < 7) {
#pragma unroll
      for (int j = 0; j < 3; ++j) *(LDS_AS f32x4*)(&scr[256 + wm * 96 + acc_col(ct, wn, j) + g * 4]) = cs[j];
    } else if (wn == 1) {
#pragma unroll
      for (int j = 0; j < 3; ++j) *(LDS_AS f32x4*)(&scr[256 + wm * 96 + j * 16 + g * 4]) = cs[j];
    }
  }
  __syncthreads();
  const float rsum = scr[arow] + scr[64 + arow], rsq = scr[128 + arow] + scr[192 + arow];
  const float mean = rsum * (1.0f / D_);
  const float var = fmaxf(rsq * (1.0f / D_) - mean * mean, 0.0f);
  const float rstd = rsqrtf(var + LN_EPS);
  const int row = row0 + arow;
  if (ct < 7) {
    u16* Q = (u16*)(ws + WS_Q);
    const int n0 = ct * 96;
#pragma unroll
    for (int j = 0; j < 3; ++j) {
      if (ct == 6 && j == 2) continue;
      int nl = acc_col(ct, wn, j) + g * 4;
      f32x4 c = *(LDS_AS f32x4*)&scr[256 + nl];
      c += *(LDS_AS f32x4*)&scr[256 + 96 + nl];
      c += *(LDS_AS f32x4*)&scr[256 + 192 + nl];
      c += *(LDS_AS f32x4*)&scr[256 + 288 + nl];
      float v[4];
#pragma unroll
      for (int r = 0; r < 4; ++r) v[r] = (acc[j][r] - mean * c[r]) * (rstd * QSCALE);
      u32x2 o = {pk_bf16(v[0], v[1]), pk_bf16(v[2], v[3])};
      *(u32x2*)(Q + (size_t)row * D_ + n0 + nl) = o;
    }
  } else if (wn == 0) {
    char* kv = ws + WS_KV + (size_t)(row >> 5) * KVT;
    const int kk = row & 31;
    float ksq = 0.f;
#pragma unroll
    for (int j = 0; j < 3; ++j) {
      int nl = j * 16 + g * 4;
      if (nl >= 40) continue;
      f32x4 c = *(LDS_AS f32x4*)&scr[256 + nl];
      c += *(LDS_AS f32x4*)&scr[256 + 96 + nl];
      c += *(LDS_AS f32x4*)&scr[256 + 192 + nl];
      c += *(LDS_AS f32x4*)&scr[256 + 288 + nl];
      float v[4];
#pragma unroll
      for (int r = 0; r < 4; ++r) v[r] = (acc[j][r] - mean * c[r]) * rstd;
      u32x2 o = {pk_bf16(v[0], v[1]), pk_bf16(v[2], v[3])};
      if (nl < 20) {
        float a0 = bf_lo(o[0]), a1 = bf_hi(o[0]), a2 = bf_lo(o[1]), a3 = bf_hi(o[1]);
        ksq += a0 * a0 + a1 * a1 + a2 * a2 + a3 * a3;
        if (nl < 16) {
          *(u32x2*)(kv + ((nl >> 3) * 32 + kk) * 16 + (nl & 7) * 2) = o;
        } else {
          *(u32x2*)(kv + 1024 + kk * 8) = o;
          u32x2 cst = {0x3f803f80u, 0x00003f80u};
          *(u32x2*)(kv + 1024 + 256 + kk * 8) = cst;
        }
      } else {
        int d = nl - 20;
        int s = kk >> 4, k16 = kk & 15;
        int h = (k16 >> 2) & 1, jj = ((k16 >> 3) << 2) | (k16 & 3);
        u16* vt = (u16*)(kv + 1536) + ((s * 2 + h) * 21 + d) * 8 + jj;
        vt[0] = (u16)(o[0] & 0xffff);
        vt[8] = (u16)(o[0] >> 16);
        vt[16] = (u16)(o[1] & 0xffff);
        vt[24] = (u16)(o[1] >> 16);
        if (d == 16) vt[32] = (u16)0x3f80;
      }
    }
    ksq += __shfl_xor(ksq, 16);
    ksq += __shfl_xor(ksq, 32);
#pragma unroll
    for (int o = 1; o < 16; o <<= 1) ksq = fmaxf(ksq, __shfl_xor(ksq, o));
    if (lane == 0) ((float*)(ws + WS_KMAX))[rb * 4 + wm] = ksq;
  }
  STAMP(ws, 0, 2);
}

constexpr int AT_STAGE = 8 * KVT;
constexpr int AT_LDS = 4 * AT_STAGE;

__device__ __forceinline__ void split3(float m, unsigned& w0, unsigned& w1) {
  float a = -m;
  unsigned ua = __builtin_bit_cast(unsigned, a) & 0xffff0000u;
  float r1 = a - __builtin_bit_cast(float, ua);
  unsigned ub = __builtin_bit_cast(unsigned, r1) & 0xffff0000u;
  float r2 = r1 - __builtin_bit_cast(float, ub);
  unsigned uc = __builtin_bit_cast(unsigned, r2) & 0xffff0000u;
  w0 = (ua >> 16) | ub;
  w1 = uc >> 16;
}

__device__ __forceinline__ void attn_tile(const char* kt, int lane, int voff, const bf16x8 (&q0)[2], const s16x4 (&q1)[2],
                                          f32x16 (&o)[2]) {
  bf16x8 k0 = lds_b128(kt + lane * 16);
  s16x4 k1 = *(const LDS_AS s16x4*)(kt + 1024 + lane * 8);
  bf16x8 v0 = lds_b128(kt + 1536 + voff);
  bf16x8 v1 = lds_b128(kt + 1536 + 672 + voff);
#pragma unroll
  for (int t = 0; t < 2; ++t) {
    f32x16 z = {};
    f32x16 s = __builtin_amdgcn_mfma_f32_32x32x16_bf16(k0, q0[t], z, 0, 0, 0);
    s = __builtin_amdgcn_mfma_f32_32x32x8bf16_1k(k1, q1[t], s, 0, 0, 0);
    u32x4 p0, p1;
#pragma unroll
    for (int i = 0; i < 4; ++i) {
      p0[i] = pk_bf16(__builtin_amdgcn_exp2f(s[2 * i]), __builtin_amdgcn_exp2f(s[2 * i + 1]));
      p1[i] = pk_bf16(__builtin_amdgcn_exp2f(s[8 + 2 * i]), __builtin_amdgcn_exp2f(s[9 + 2 * i]));
    }
    o[t] = __builtin_amdgcn_mfma_f32_32x32x16_bf16(v0, __builtin_bit_cast(bf16x8, p0), o[t], 0, 0, 0);
    o[t] = __builtin_amdgcn_mfma_f32_32x32x16_bf16(v1, __builtin_bit_cast(bf16x8, p1), o[t], 0, 0, 0);
  }
}

__global__ __launch_bounds__(512, 2) void k2_attn(const u16* __restrict__ wg, const u16* __restrict__ wu,
                                                  char* __restrict__ ws) {
  extern __shared__ __attribute__((aligned(16))) char smem[];
  const int tid = threadIdx.x, lane = tid & 63;
  const int wave = __builtin_amdgcn_readfirstlane(tid >> 6);
  const int grp = wave >> 2, wq = wave & 3;
  const int r = lane & 31, h = lane >> 5;
  const char* kvg = ws + WS_KV;
  const int pos0 = blockIdx.x * 8 + wq * 2;

  unsigned kvo[6];
#pragma unroll
  for (int it = 0; it < 6; ++it) {
    int c = it * 512 + wave * 64 + lane;
    int gg = c >= 1440;
    kvo[it] = (unsigned)(gg * 4 * AT_STAGE + (c - gg * 1440) * 16);
  }
  const unsigned l0 = lds_addr(smem);
  auto issue = [&](int st, int buf) {
#pragma unroll
    for (int it = 0; it < 6; ++it) {
      if (it < 5 || wave < 5) glds16s(kvo[it], kvg + (size_t)st * AT_STAGE, l0 + buf * 2 * AT_STAGE + (it * 512 + wave * 64) * 16);
    }
  };
  STAMP(ws, 1, 0);
  issue(0, 0);

  {
    const int c0 = blockIdx.x * 12;
    const u16* w = (c0 < F_) ? (wg + c0) : (wu + (c0 - F_));
    float a[12];
#pragma unroll
    for (int i = 0; i < 12; ++i) a[i] = 0.f;
#pragma unroll
    for (int it = 0; it < 2; ++it) {
      int k = tid + it * 512;
      if (k < D_) {
        const u32x2* s = (const u32x2*)(w + (size_t)k * F_);
        u32x2 v0 = s[0], v1 = s[1], v2 = s[2];
        a[0] += bf_lo(v0[0]); a[1] += bf_hi(v0[0]); a[2] += bf_lo(v0[1]); a[3] += bf_hi(v0[1]);
        a[4] += bf_lo(v1[0]); a[5] += bf_hi(v1[0]); a[6] += bf_lo(v1[1]); a[7] += bf_hi(v1[1]);
        a[8] += bf_lo(v2[0]); a[9] += bf_hi(v2[0]); a[10] += bf_lo(v2[1]); a[11] += bf_hi(v2[1]);
      }
    }
#pragma unroll
    for (int i = 0; i < 12; ++i) {
#pragma unroll
      for (int o = 1; o < 64; o <<= 1) a[i] += __shfl_xor(a[i], o);
    }
    float* red = (float*)(smem + AT_LDS);
    if (lane == 0) {
#pragma unroll
      for (int i = 0; i < 12; ++i) red[wave * 12 + i] = a[i];
    }
  }

  float kmax2;
  {
    const float* km = (const float*)(ws + WS_KMAX);
    float m = fmaxf(km[lane], km[lane + 64]);
#pragma unroll
    for (int o = 1; o < 64; o <<= 1) m = fmaxf(m, __shfl_xor(m, o));
    kmax2 = m;
  }
  bf16x8 q0[2];
  s16x4 q1[2];
  u32x2 qtail[2];
  float mshift[2];
  const u16* Qs = (const u16*)(ws + WS_Q);
#pragma unroll
  for (int t = 0; t < 2; ++t) {
    const u16* qp = Qs + (size_t)(pos0 + t) * D_ + r * 20;
    u32x2 a = *(const u32x2*)(qp + 8 * h), b2 = *(const u32x2*)(qp + 8 * h + 4);
    u32x4 qq = {a[0], a[1], b2[0], b2[1]};
    q0[t] = __builtin_bit_cast(bf16x8, qq);
    u32x2 tl = *(const u32x2*)(qp + 16);
    qtail[t] = tl;
    float ss = 0.f;
#pragma unroll
    for (int i = 0; i < 4; ++i) { float x0 = bf_lo(qq[i]), x1 = bf_hi(qq[i]); ss += x0 * x0 + x1 * x1; }
    if (h == 0) {
#pragma unroll
      for (int i = 0; i < 2; ++i) { float x0 = bf_lo(tl[i]), x1 = bf_hi(tl[i]); ss += x0 * x0 + x1 * x1; }
    }
    ss += __shfl_xor(ss, 32);
    mshift[t] = sqrtf(ss * kmax2) * 1.0001f;
  }

  const int voff = (h * 21 + (r < 21 ? r : 20)) * 16;
  f32x16 o[2];
  float* xch = (float*)smem;
  int* flag = (int*)(smem + AT_LDS + 512);

  for (int attempt = 0;; ++attempt) {
#pragma unroll
    for (int t = 0; t < 2; ++t) {
      unsigned w0, w1;
      split3(mshift[t], w0, w1);
      u32x2 f = (h == 0) ? qtail[t] : u32x2{w0, w1};
      q1[t] = __builtin_bit_cast(s16x4, f);
#pragma unroll
      for (int i = 0; i < 16; ++i) o[t][i] = 0.f;
    }
    if (attempt) issue(0, 0);
    STAMP(ws, 1, 1);
#pragma unroll 1
    for (int st = 0; st < 4; ++st) {
      wait_vm<0>();
      bar();
      if (st < 3) issue(st + 1, (st + 1) & 1);
      const char* kb = smem + (st & 1) * 2 * AT_STAGE + grp * AT_STAGE;
#pragma unroll
      for (int kt = 0; kt < 8; ++kt) attn_tile(kb + kt * KVT, lane, voff, q0, q1, o);
    }
    __syncthreads();
    STAMP(ws, 1, 2);
    if (grp == 1) {
#pragma unroll
      for (int t = 0; t < 2; ++t)
#pragma unroll
        for (int i = 0; i < 12; ++i) xch[((wq * 2 + t) * 12 + i) * 64 + lane] = o[t][i];
    }
    if (tid == 0) *flag = 0;
    __syncthreads();
    bool bad = false;
    float linv[2];
    if (grp == 0) {
#pragma unroll
      for (int t = 0; t < 2; ++t) {
#pragma unroll
        for (int i = 0; i < 12; ++i) o[t][i] += xch[((wq * 2 + t) * 12 + i) * 64 + lane];
        float l = __shfl(o[t][8], 32 + r);
        bad |= !(l >= 5.421010862427522e-20f);
        linv[t] = 1.0f / l;
      }
      if (attempt == 0 && bad) *flag = 1;
    }
    __syncthreads();
    if (attempt == 1 || *flag == 0) {
      if (grp == 0) {
        u16* O = (u16*)(ws + WS_O);
#pragma unroll
        for (int t = 0; t < 2; ++t) {
          u16* op = O + (size_t)(pos0 + t) * D_ + r * 20 + 4 * h;
#pragma unroll
          for (int gq = 0; gq < 3; ++gq) {
            if (gq == 2 && h == 1) continue;
            u32x2 v = {pk_bf16(o[t][4 * gq] * linv[t], o[t][4 * gq + 1] * linv[t]),
                       pk_bf16(o[t][4 * gq + 2] * linv[t], o[t][4 * gq + 3] * linv[t])};
            *(u32x2*)(op + 8 * gq) = v;
          }
        }
      }
      break;
    }
    __syncthreads();
#pragma unroll 1
    for (int t = 0; t < 2; ++t) {
      const u16* qp = Qs + (size_t)(pos0 + t) * D_ + r * 20;
      float qf[20];
#pragma unroll
      for (int i = 0; i < 10; ++i) { unsigned u = *(const unsigned*)(qp + 2 * i); qf[2 * i] = bf_lo(u); qf[2 * i + 1] = bf_hi(u); }
      float mx = -INFINITY;
#pragma unroll 1
      for (int kk = 0; kk < 512; ++kk) {
        int key = grp * 1024 + h * 512 + kk;
        const char* t0 = kvg + (size_t)(key >> 5) * KVT;
        int kr = key & 31;
        u32x4 a = *(const u32x4*)(t0 + kr * 16), b2 = *(const u32x4*)(t0 + 512 + kr * 16);
        u32x2 c = *(const u32x2*)(t0 + 1024 + kr * 8);
        float sacc = 0.f;
#pragma unroll
        for (int i = 0; i < 4; ++i) {
          sacc += qf[2 * i] * bf_lo(a[i]) + qf[2 * i + 1] * bf_hi(a[i]);
          sacc += qf[8 + 2 * i] * bf_lo(b2[i]) + qf[9 + 2 * i] * bf_hi(b2[i]);
        }
        sacc += qf[16] * bf_lo(c[0]) + qf[17] * bf_hi(c[0]) + qf[18] * bf_lo(c[1]) + qf[19] * bf_hi(c[1]);
        mx = fmaxf(mx, sacc);
      }
      mx = fmaxf(mx, __shfl_xor(mx, 32));
      if (h == 0) xch[(grp * 8 + wq * 2 + t) * 32 + r] = mx;
    }
    __syncthreads();
#pragma unroll
    for (int t = 0; t < 2; ++t) mshift[t] = fmaxf(xch[(wq * 2 + t) * 32 + r], xch[(8 + wq * 2 + t) * 32 + r]);
    __syncthreads();
  }
  if (tid < 12) {
    const float* red = (const float*)(smem + AT_LDS);
    float s = 0.f;
#pragma unroll
    for (int w = 0; w < 8; ++w) s += red[w * 12 + tid];
    ((float*)(ws + WS_CS))[blockIdx.x * 12 + tid] = s;
  }
  STAMP(ws, 1, 3);
}

constexpr int G2_RING = 7;
constexpr int G2_LDS = G2_RING * ST64 + 256;

template <int K, bool STATS>
__global__ __launch_bounds__(512, 2) void k_gemm96(const u16* __restrict__ A, const u16* __restrict__ W,
                                                   const u16* __restrict__ res, u16* __restrict__ out,
                                                   float* __restrict__ stats, char* __restrict__ ws) {
  extern __shared__ __attribute__((aligned(16))) char smem[];
  const int tid = threadIdx.x, lane = tid & 63;
  const int wave = __builtin_amdgcn_readfirstlane(tid >> 6);
  const int wm = wave & 3, wn = wave >> 2;
  const int b = blockIdx.x;
  const int xcd = b & 7, idx = b >> 3;
  const int rb = xcd * 4 + (idx & 3), ct = idx >> 2;
  const int row0 = rb * 64, n0 = ct * 96;
  const int g = lane >> 4;
  const int arow = wm * 16 + (lane & 15);
  const int row = row0 + arow;
  const int nj = (ct == 6) ? 2 : 3;
  STAMP(ws, STATS ? 2 : 4, 0);
  if (PF) pf_regions<(K > 640 ? 2 : 1)>((const char*)(A + (size_t)xcd * 256 * K), 256 * K * 2 / 128, (const char*)W, K * D_ * 2 / 128, idx,
                                28, wave, lane, lds_addr(smem) + G2_RING * ST64);

  u32x2 rv[3];
#pragma unroll
  for (int j = 0; j < 3; ++j) {
    int c = (j < nj) ? acc_col(ct, wn, j) : 0;
    rv[j] = *(const u32x2*)(res + (size_t)row * D_ + n0 + c + g * 4);
  }
  f32x4 acc[3] = {}, st[2] = {}, cs[3] = {};
  gemm64<K / 64, G2_RING, false>(smem, A + (size_t)row0 * K, K, W + n0, D_, ct, wave, lane, acc, st, cs);
  STAMP(ws, STATS ? 2 : 4, 1);

  float sum = 0.f;
  float hv[3][4];
#pragma unroll
  for (int j = 0; j < 3; ++j) {
    if (j < nj) {
      float v0 = acc[j][0] + bf_lo(rv[j][0]), v1 = acc[j][1] + bf_hi(rv[j][0]);
      float v2 = acc[j][2] + bf_lo(rv[j][1]), v3 = acc[j][3] + bf_hi(rv[j][1]);
      u32x2 o = {pk_bf16(v0, v1), pk_bf16(v2, v3)};
      *(u32x2*)(out + (size_t)row * D_ + n0 + acc_col(ct, wn, j) + g * 4) = o;
      if (STATS) {
        hv[j][0] = bf_lo(o[0]); hv[j][1] = bf_hi(o[0]); hv[j][2] = bf_lo(o[1]); hv[j][3] = bf_hi(o[1]);
        sum += hv[j][0] + hv[j][1] + hv[j][2] + hv[j][3];
      }
    } else if (STATS) {
      hv[j][0] = hv[j][1] = hv[j][2] = hv[j][3] = 0.f;
    }
  }
  if (STATS) {
    float* scr = (float*)smem;
    sum += __shfl_xor(sum, 16);
    sum += __shfl_xor(sum, 32);
    __syncthreads();
    if (g == 0) scr[wn * 64 + arow] = sum;
    __syncthreads();
    const float tot = scr[arow] + scr[64 + arow];
    const float mu = tot * ((ct == 6) ? (1.0f / 64.0f) : (1.0f / 96.0f));
    float m2 = 0.f;
#pragma unroll
    for (int j = 0; j < 3; ++j) {
      if (j < nj) {
#pragma unroll
        for (int r = 0; r < 4; ++r) { float d = hv[j][r] - mu; m2 += d * d; }
      }
    }
    m2 += __shfl_xor(m2, 16);
    m2 += __shfl_xor(m2, 32);
    if (g == 0) scr[128 + wn * 64 + arow] = m2;
    __syncthreads();
    if (wn == 0 && g == 0) {
      f32x2 o = {tot, scr[128 + arow] + scr[192 + arow]};
      *(f32x2*)(stats + ((size_t)row * 8 + ct) * 2) = o;
    }
  }
  STAMP(ws, STATS ? 2 : 4, 2);
}

constexpr int G3_RING = 4;
constexpr int G3_LDS = G3_RING * 40 * 1024;

__global__ __launch_bounds__(512, 2) void k4_ffn_up(const u16* __restrict__ Hh, const u16* __restrict__ wg,
                                                    const u16* __restrict__ wu, const float* __restrict__ stats,
                                                    const float* __restrict__ cs, u16* __restrict__ act,
                                                    char* __restrict__ ws) {
  extern __shared__ __attribute__((aligned(16))) char smem[];
  constexpr int ST = 40 * 1024;
  const int tid = threadIdx.x, lane = tid & 63;
  const int wave = __builtin_amdgcn_readfirstlane(tid >> 6);
  const int wm = wave & 3, wn = wave >> 2;
  const int b = blockIdx.x;
  const int xcd = b & 7, idx = b >> 3;
  const int rb = (xcd & 1) * 8 + (idx & 7);
  const int ct = (xcd >> 1) * 4 + (idx >> 3);
  const int row0 = rb * 128, n0 = ct * 96;
  const int g = lane >> 4;
  STAMP(ws, 3, 0);
  if (PF) {
    const char* hb = (const char*)(Hh + (size_t)(xcd & 1) * 1024 * D_);
    const int wc0 = (xcd >> 1) * 384;
#pragma unroll
    for (int t = 0; t < 2; ++t) {
      int i0 = (wave + t * 8) * 64;
      if (i0 < 560) {
        int l = min(idx * 560 + i0 + lane, 17919);
        int lw = l - 10240;
        int m = lw >= 3840;
        lw -= m * 3840;
        int k = lw / 6, c = lw - k * 6;
        const char* wp = (const char*)((m ? wu : wg) + (size_t)k * F_ + wc0) + c * 128;
        pf_line(l < 10240 ? hb + (size_t)l * 128 : wp, lds_addr(smem) + 3 * ST + ST - 256);
      }
    }
  }

  const f32x4* sp = (const f32x4*)(stats + (size_t)(row0 + (tid & 127)) * 16);
  const f32x4 s0 = sp[0], s1 = sp[1], s2 = sp[2];
  const f32x2 s3 = *(const f32x2*)(sp + 3);
  const int c64 = (ct & 1) ? 32 : 0, c32 = (ct & 1) ? 0 : 64;
  const unsigned va = voff_a(wave * 16, lane, D_);
  const unsigned v64 = voff_g64(wave, lane, F_, c64), v32 = voff_g32(wave & 3, lane, F_, c32);
  const char* abase = (const char*)(Hh + (size_t)row0 * D_);
  const char* gbase = (const char*)(wg + n0);
  const char* ubase = (const char*)(wu + n0);
  const unsigned l0 = lds_addr(smem);
  auto issue = [&](int s, int slot) {
    const unsigned d = l0 + slot * ST;
    const char* gw = gbase + (size_t)s * 64 * F_ * 2;
    const char* uw = ubase + (size_t)s * 64 * F_ * 2;
    glds16s(va, abase + s * 128, d + wave * 2048);
    glds16s(va, abase + s * 128 + 8 * D_ * 2, d + wave * 2048 + 1024);
    glds16s(v64, gw, d + 16384 + wave * 1024);
    glds16s(v64, uw, d + 24576 + wave * 1024);
    glds16s(v32, wave < 4 ? gw : uw, d + 32768 + wave * 1024);
  };
#pragma unroll
  for (int s = 0; s < G3_RING - 1; ++s) issue(s, s);

  const int wo0 = 16384 + roff_g64(wn * 2, lane), wo1 = 16384 + roff_g64(wn * 2 + 1, lane), wo2 = 32768 + roff_g32(wn, lane);
  const int ar0 = wm * 32 + (lane & 15);
  f32x4 ag[2][3] = {}, au[2][3] = {};
  struct Frag { bf16x8 a[2], g[3], u[3]; };
  const int ao0 = ar0 * 128 + ((g ^ (ar0 & 7)) << 4), ao1 = ar0 * 128 + (((4 + g) ^ (ar0 & 7)) << 4);
  auto rd = [&](Frag& f, const char* buf, int ks) {
    f.a[0] = lds_b128(buf + (ks ? ao1 : ao0));
    f.g[0] = rd_g64(buf + wo0, ks);
    f.u[0] = rd_g64(buf + wo0 + 8192, ks);
    f.a[1] = lds_b128(buf + (ks ? ao1 : ao0) + 2048);
    f.g[1] = rd_g64(buf + wo1, ks);
    f.u[1] = rd_g64(buf + wo1 + 8192, ks);
    f.g[2] = rd_g32(buf + wo2, ks);
    f.u[2] = rd_g32(buf + wo2 + 4096, ks);
  };
  auto mm = [&](const Frag& f) {
#pragma unroll
    for (int j = 0; j < 3; ++j)
#pragma unroll
      for (int mi = 0; mi < 2; ++mi) {
        ag[mi][j] = mfma16(f.g[j], f.a[mi], ag[mi][j]);
        au[mi][j] = mfma16(f.u[j], f.a[mi], au[mi][j]);
      }
  };
  Frag f0, f1;
  wait_vm<10>();
  bar();
  STAMP2(3, 0);
  rd(f0, smem, 0);
#pragma unroll
  for (int s = 0; s < 10; ++s) {
    const char* buf = smem + (s % G3_RING) * ST;
    rd(f1, buf, 1);
    __builtin_amdgcn_sched_barrier(0);
    mm(f0);
    __builtin_amdgcn_sched_barrier(0);
    if (s < 9) {
      if (s < 8) wait_vm<5>();
      else wait_vm<0>();
      bar();
      STAMP2(3, s + 1);
      if (s + 3 < 10) issue(s + 3, (s + 3) % G3_RING);
      rd(f0, smem + ((s + 1) % G3_RING) * ST, 0);
      __builtin_amdgcn_sched_barrier(0);
    }
    mm(f1);
    __builtin_amdgcn_sched_barrier(0);
  }
  bar();
  STAMP(ws, 3, 1);
  float* scr = (float*)smem;
  float mean_r = 0.f, rstd_r = 0.f;
  if (tid < 128) {
    float tot = s0[0] + s0[2] + s1[0] + s1[2] + s2[0] + s2[2] + s3[0];
    float mean = tot * (1.0f / D_);
    float m2 = s0[1] + s0[3] + s1[1] + s1[3] + s2[1] + s2[3] + s3[1];
    float c = 0.f, d;
    d = s0[0] * (1.f / 96.f) - mean; c += d * d; d = s0[2] * (1.f / 96.f) - mean; c += d * d;
    d = s1[0] * (1.f / 96.f) - mean; c += d * d; d = s1[2] * (1.f / 96.f) - mean; c += d * d;
    d = s2[0] * (1.f / 96.f) - mean; c += d * d; d = s2[2] * (1.f / 96.f) - mean; c += d * d;
    d = s3[0] * (1.f / 64.f) - mean;
    float var = (m2 + 96.f * c + 64.f * d * d) * (1.0f / D_);
    mean_r = mean;
    rstd_r = rsqrtf(var + LN_EPS);
  }

  if (tid < 128) { scr[tid * 2] = mean_r; scr[tid * 2 + 1] = rstd_r; }
  __syncthreads();
#pragma unroll
  for (int mi = 0; mi < 2; ++mi) {
    const int ar = ar0 + mi * 16;
    const f32x2 ms = *(LDS_AS f32x2*)&scr[ar * 2];
    const float rstd = ms[1], nm = -ms[0] * ms[1];
#pragma unroll
    for (int j = 0; j < 3; ++j) {
      const int n = n0 + acc_col(ct, wn, j) + g * 4;
      const f32x4 cg = *(const f32x4*)(cs + n), cu = *(const f32x4*)(cs + F_ + n);
      float o[4];
#pragma unroll
      for (int r = 0; r < 4; ++r) {
        float gv = ag[mi][j][r] * rstd + nm * cg[r];
        float uv = au[mi][j][r] * rstd + nm * cu[r];
        float t = gv * gv * (0.044715f * 1.5957691216057308f * 1.4426950408889634f) + (1.5957691216057308f * 1.4426950408889634f);
        float e = __builtin_amdgcn_exp2f(-gv * t);
        o[r] = gv * uv * __builtin_amdgcn_rcpf(1.0f + e);
      }
      u32x2 v = {pk_bf16(o[0], o[1]), pk_bf16(o[2], o[3])};
      *(u32x2*)(act + (size_t)(row0 + ar) * F_ + n) = v;
    }
  }
  STAMP(ws, 3, 2);
}

extern "C" void kernel_launch(void* const* d_in, const int* in_sizes, int n_in, void* d_out, int out_size,
                              void* d_ws, size_t ws_size, hipStream_t stream) {
  const u16* x = (const u16*)d_in[0];
  const u16* wq = (const u16*)d_in[1];
  const u16* wk = (const u16*)d_in[2];
  const u16* wv = (const u16*)d_in[3];
  const u16* wo = (const u16*)d_in[4];
  const u16* wg = (const u16*)d_in[6];
  const u16* wu = (const u16*)d_in[7];
  const u16* wd = (const u16*)d_in[8];
  char* ws = (char*)d_ws;
  u16* out = (u16*)d_out;

  (void)hipFuncSetAttribute((const void*)k1_qkv, hipFuncAttributeMaxDynamicSharedMemorySize, K1_LDS);
  (void)hipFuncSetAttribute((const void*)k2_attn, hipFuncAttributeMaxDynamicSharedMemorySize, AT_LDS + 1024);
  (void)hipFuncSetAttribute((const void*)k_gemm96<640, true>, hipFuncAttributeMaxDynamicSharedMemorySize, G2_LDS);
  (void)hipFuncSetAttribute((const void*)k_gemm96<1536, false>, hipFuncAttributeMaxDynamicSharedMemorySize, G2_LDS);
  (void)hipFuncSetAttribute((const void*)k4_ffn_up, hipFuncAttributeMaxDynamicSharedMemorySize, G3_LDS);

  k1_qkv<<<256, 512, K1_LDS, stream>>>(x, wq, wk, wv, ws);
  k2_attn<<<256, 512, AT_LDS + 1024, stream>>>(wg, wu, ws);
  k_gemm96<640, true><<<224, 512, G2_LDS, stream>>>((const u16*)(ws + WS_O), wo, x, (u16*)(ws + WS_H),
                                                    (float*)(ws + WS_STAT), ws);
  k4_ffn_up<<<256, 512, G3_LDS, stream>>>((const u16*)(ws + WS_H), wg, wu, (const float*)(ws + WS_STAT),
                                          (const float*)(ws + WS_CS), (u16*)(ws + WS_ACT), ws);
  k_gemm96<1536, false><<<224, 512, G2_LDS, stream>>>((const u16*)(ws + WS_ACT), wd, (const u16*)(ws + WS_H), out,
                                                      (float*)(ws + WS_STAT), ws);
}
```

```cpp
#include <hip/hip_runtime.h>
#include <hip/hip_bf16.h>

typedef __attribute__((ext_vector_type(8))) __bf16 bf16x8;
typedef __attribute__((ext_vector_type(4))) __bf16 bf16x4;
typedef __attribute__((ext_vector_type(2))) __bf16 bf16x2;
typedef __attribute__((ext_vector_type(4))) short s16x4;
typedef __attribute__((ext_vector_type(16))) float f32x16;
typedef __attribute__((ext_vector_type(4))) float f32x4;
typedef __attribute__((ext_vector_type(2))) float f32x2;
typedef __attribute__((ext_vector_type(4))) unsigned u32x4;
typedef __attribute__((ext_vector_type(2))) unsigned u32x2;
typedef unsigned short u16;

#define LDS_AS __attribute__((address_space(3)))
#define GLB_AS __attribute__((address_space(1)))

#define PF 0
constexpr int S_ = 2048, D_ = 640, F_ = 1536;
constexpr float LN_EPS = 1e-5f;
constexpr float QSCALE = 0.22360679774997896f * 1.4426950408889634f;

constexpr int KVT = 2880;
constexpr int KV_BYTES = 64 * KVT;

constexpr size_t WS_Q = 0;
constexpr size_t WS_KV = WS_Q + (size_t)S_ * D_ * 2;
constexpr size_t WS_KMAX = WS_KV + KV_BYTES;
constexpr size_t WS_O = WS_KMAX + 512;
constexpr size_t WS_H = WS_O + (size_t)S_ * D_ * 2;
constexpr size_t WS_STAT = WS_H + (size_t)S_ * D_ * 2;
constexpr size_t WS_CS = WS_STAT + (size_t)S_ * 8 * 2 * 4;
constexpr size_t WS_ACT = WS_CS + 3072 * 4;

__device__ __forceinline__ unsigned pk_bf16(float a, float b) {
  f32x2 f = {a, b};
  return __builtin_bit_cast(unsigned, __builtin_convertvector(f, bf16x2));
}
__device__ __forceinline__ float bf_lo(unsigned u) { return __builtin_bit_cast(float, u << 16); }
__device__ __forceinline__ float bf_hi(unsigned u) { return __builtin_bit_cast(float, u & 0xffff0000u); }

#define STAMP(ws_, kid, i)
#define STAMP2(kid, i)
template <int N> __device__ __forceinline__ void wait_vm() { asm volatile("s_waitcnt vmcnt(%0)" ::"i"(N) : "memory"); }
__device__ __forceinline__ void bar() { __builtin_amdgcn_s_barrier(); }
__device__ __forceinline__ void glds16s(unsigned voff, const void* sbase, unsigned lds) {
  asm volatile("s_mov_b32 m0, %2\n\ts_nop 3\n\tglobal_load_lds_dwordx4 %0, %1" ::"v"(voff), "s"(sbase), "s"(lds)
               : "memory");
}
__device__ __forceinline__ void pf_line(const char* addr, unsigned lds_dump) {
  asm volatile("s_mov_b32 m0, %1\n\ts_nop 3\n\tglobal_load_lds_dword %0, off" ::"v"(addr), "s"(lds_dump) : "memory");
}
template <int NI>
__device__ __forceinline__ void pf_regions(const char* A, int nA, const char* B, int nB, int part, int nparts, int wave,
                                           int lane, unsigned lds_dump) {
  const int per = (nA + nB + nparts - 1) / nparts;
#pragma unroll
  for (int t = 0; t < NI; ++t) {
    int i0 = (wave + t * 8) * 64;
    if (i0 < per) {
      int l = part * per + i0 + lane;
      l = min(l, nA + nB - 1);
      pf_line(l < nA ? A + (size_t)l * 128 : B + (size_t)(l - nA) * 128, lds_dump);
    }
  }
}
#define WT 0
__device__ __forceinline__ void st_out(void* p, u32x2 v) {
  *(u32x2*)p = v;
}
__device__ __forceinline__ unsigned lds_addr(const char* p) { return (unsigned)(size_t)(const LDS_AS char*)p; }
__device__ __forceinline__ void lds_fence() { asm volatile("s_waitcnt lgkmcnt(0)" ::: "memory"); }
__device__ __forceinline__ bf16x8 lds_b128(const char* p) { return *(const LDS_AS bf16x8*)(p); }
__device__ __forceinline__ bf16x8 lds_tr_pair(const char* p0, const char* p1) {
  bf16x4 a = __builtin_amdgcn_ds_read_tr16_b64_v4bf16((LDS_AS bf16x4*)p0);
  bf16x4 b = __builtin_amdgcn_ds_read_tr16_b64_v4bf16((LDS_AS bf16x4*)p1);
  return __builtin_shufflevector(a, b, 0, 1, 2, 3, 4, 5, 6, 7);
}
__device__ __forceinline__ f32x4 mfma16(bf16x8 a, bf16x8 b, f32x4 c) {
  return __builtin_amdgcn_mfma_f32_16x16x32_bf16(a, b, c, 0, 0, 0);
}
__device__ __forceinline__ bf16x8 ones8() {
  u32x4 o = {0x3f803f80u, 0x3f803f80u, 0x3f803f80u, 0x3f803f80u};
  return __builtin_bit_cast(bf16x8, o);
}

__device__ __forceinline__ unsigned voff_a(int row, int lane, int lda) {
  return (unsigned)((row + (lane >> 3)) * lda + (((lane & 7) ^ (lane >> 3)) << 3)) * 2u;
}
__device__ __forceinline__ unsigned voff_g64(int kb, int lane, int ldw, int c64) {
  int r = lane >> 3, pos = lane & 7;
  int u = (pos >> 1) ^ (((r >> 1) & 1) | ((kb & 1) << 1));
  return (unsigned)((kb * 8 + r) * ldw + c64 + u * 16 + (pos & 1) * 8) * 2u;
}
__device__ __forceinline__ unsigned voff_g32(int kbp, int lane, int ldw, int c32) {
  int kb = kbp * 2 + (lane >> 5), r = (lane >> 2) & 7, pos = lane & 3;
  int u = (pos >> 1) ^ (kb & 1);
  return (unsigned)((kb * 8 + r) * ldw + c32 + u * 16 + (pos & 1) * 8) * 2u;
}
__device__ __forceinline__ int roff_g64(int u, int lane) {
  int g = lane >> 4, q = (lane & 15) >> 2, p = lane & 3;
  return g * 1024 + q * 128 + ((u ^ (((q >> 1) & 1) | ((g & 1) << 1))) << 5) + p * 8;
}
__device__ __forceinline__ int roff_g32(int v, int lane) {
  int g = lane >> 4, q = (lane & 15) >> 2, p = lane & 3;
  return g * 512 + q * 64 + ((v ^ (g & 1)) << 5) + p * 8;
}
__device__ __forceinline__ bf16x8 rd_g64(const char* p, int ks) { return lds_tr_pair(p + ks * 4096, p + ks * 4096 + 512); }
__device__ __forceinline__ bf16x8 rd_g32(const char* p, int ks) { return lds_tr_pair(p + ks * 2048, p + ks * 2048 + 256); }

constexpr int ST64 = 20 * 1024;

struct Frag64 { bf16x8 a[2], w[3]; };
template <int NS, int RING, int NPW, bool HAS32, bool QKV>
__device__ __forceinline__ void gemm64_loop(char* smem, const char* abase, const char* wbase, int ldw2, unsigned va,
                                            unsigned v64, unsigned v32, int wave, int lane, f32x4 (&out)[3], f32x4 (&st)[2],
                                            f32x4 (&cs)[3]) {
  const int kh = wave & 1, wm2 = (wave >> 1) & 1, wn = wave >> 2, g = lane >> 4;
  const unsigned l0 = lds_addr(smem);
  const int arow = wm2 * 32 + (lane & 15);
  const int ao = arow * 128 + (((kh * 4 + g) ^ (arow & 7)) << 4);
  const int wo0 = 8192 + kh * 4096 + roff_g64(wn * 2, lane), wo1 = 8192 + kh * 4096 + roff_g64(wn * 2 + 1, lane);
  const int wo2 = 16384 + kh * 2048 + roff_g32(wn, lane);
  f32x4 acc[2][3] = {};
  auto issue = [&](int s, int slot) {
    const unsigned d = l0 + slot * ST64 + wave * 1024;
    const char* w = wbase + (size_t)s * 64 * ldw2;
    glds16s(va, abase + s * 128, d);
    glds16s(v64, w, d + 8192);
    if (NPW == 3) glds16s(v32, w, d + 16384);
  };
  auto rd = [&](Frag64& f, int slot) {
    const char* buf = smem + slot * ST64;
    f.a[0] = lds_b128(buf + ao);
    f.w[0] = rd_g64(buf + wo0, 0);
    f.a[1] = lds_b128(buf + ao + 2048);
    f.w[1] = rd_g64(buf + wo1, 0);
    if (HAS32) f.w[2] = rd_g32(buf + wo2, 0);
  };
  auto mm = [&](const Frag64& f, int s) {
#pragma unroll
    for (int j = 0; j < (HAS32 ? 3 : 2); ++j) {
      acc[0][j] = mfma16(f.w[j], f.a[0], acc[0][j]);
      acc[1][j] = mfma16(f.w[j], f.a[1], acc[1][j]);
    }
    if (QKV) {
#pragma unroll
      for (int mi = 0; mi < 2; ++mi) {
        if (wn == 0) st[mi] = mfma16(ones8(), f.a[mi], st[mi]);
        else st[mi] = mfma16(f.a[mi], f.a[mi], st[mi]);
      }
      if ((s & 1) == wm2) {
#pragma unroll
        for (int j = 0; j < (HAS32 ? 3 : 2); ++j) cs[j] = mfma16(f.w[j], ones8(), cs[j]);
      }
    }
  };
  constexpr int NMAIN = NS - RING + 1;
  static_assert(NMAIN % 2 == 0 && RING == 7, "");
#pragma unroll
  for (int s = 0; s < RING - 1; ++s) issue(s, s);
  Frag64 f0, f1;
  wait_vm<NPW*(RING - 2)>();
  bar();
  STAMP2(QKV ? 0 : (NS == 10 ? 2 : 4), 0);
  rd(f0, 0);
  int slot = 0;
#define STEP(CUR, NXT, S, WAITN, ISSUE)                                   \
  {                                                                       \
    const int nslot = slot == RING - 1 ? 0 : slot + 1;                    \
    wait_vm<WAITN>();                                                     \
    bar();                                                                \
    STAMP2(QKV ? 0 : (NS == 10 ? 2 : 4), (S) + 1);                        \
    if (ISSUE) issue((S) + RING - 1, slot == 0 ? RING - 1 : slot - 1);    \
    rd(NXT, nslot);                                                       \
    __builtin_amdgcn_sched_barrier(0);                                    \
    mm(CUR, S);                                                           \
    __builtin_amdgcn_sched_barrier(0);                                    \
    slot = nslot;                                                         \
  }
#pragma unroll 1
  for (int s = 0; s < NMAIN; s += 2) {
    STEP(f0, f1, s, NPW*(RING - 3), true)
    STEP(f1, f0, s + 1, NPW*(RING - 3), true)
  }
  STEP(f0, f1, NS - 6, NPW * 4, false)
  STEP(f1, f0, NS - 5, NPW * 3, false)
  STEP(f0, f1, NS - 4, NPW * 2, false)
  STEP(f1, f0, NS - 3, NPW * 1, false)
  STEP(f0, f1, NS - 2, 0, false)
  mm(f1, NS - 1);
#undef STEP
  bar();
  f32x4* xs = (f32x4*)smem;
#pragma unroll
  for (int j = 0; j < 3; ++j) {
    f32x4 t0 = acc[0][j], t1 = acc[1][j];
    acc[0][j] = kh ? t1 : t0;
    acc[1][j] = kh ? t0 : t1;
    if (HAS32 || j < 2) *(LDS_AS f32x4*)&xs[(wave * 3 + j) * 64 + lane] = acc[1][j];
  }
  lds_fence();
  bar();
#pragma unroll
  for (int j = 0; j < 3; ++j) {
    if (HAS32 || j < 2) out[j] = acc[0][j] + *(LDS_AS f32x4*)&xs[((wave ^ 1) * 3 + j) * 64 + lane];
    else out[j] = acc[0][j];
  }
}

template <int NS, int RING, bool QKV>
__device__ __forceinline__ void gemm64(char* smem, const u16* A, int lda, const u16* W, int ldw, int ct, int wave, int lane,
                                       f32x4 (&acc)[3], f32x4 (&st)[2], f32x4 (&cs)[3]) {
  const int c64 = (ct & 1) ? 32 : 0, c32 = (ct & 1) ? 0 : 64;
  const unsigned va = voff_a(wave * 8, lane, lda);
  const unsigned v64 = voff_g64(wave, lane, ldw, c64), v32 = voff_g32(wave & 3, lane, ldw, c32);
  const char* ab = (const char*)A;
  const char* wb = (const char*)W;
  if (ct == 6) gemm64_loop<NS, RING, 2, false, QKV>(smem, ab, wb, ldw * 2, va, v64, v32, wave, lane, acc, st, cs);
  else if (wave < 4) gemm64_loop<NS, RING, 3, true, QKV>(smem, ab, wb, ldw * 2, va, v64, v32, wave, lane, acc, st, cs);
  else gemm64_loop<NS, RING, 2, true, QKV>(smem, ab, wb, ldw * 2, va, v64, v32, wave, lane, acc, st, cs);
}
__device__ __forceinline__ int acc_col(int ct, int wn, int j) {
  return j < 2 ? ((ct & 1) ? 32 : 0) + (wn * 2 + j) * 16 : ((ct & 1) ? 0 : 64) + wn * 16;
}

constexpr int K1_RING = 7;
constexpr int K1_LDS = 145 * 1024;

__global__ __launch_bounds__(512, 2) void k1_qkv(const u16* __restrict__ x, const u16* __restrict__ wq,
                                                 const u16* __restrict__ wk, const u16* __restrict__ wv,
                                                 char* __restrict__ ws) {
  extern __shared__ __attribute__((aligned(16))) char smem[];
  const int tid = threadIdx.x, lane = tid & 63;
  const int wave = __builtin_amdgcn_readfirstlane(tid >> 6);
  const int wm = wave & 3, wn = wave >> 2;
  const int b = blockIdx.x;
  const int xcd = b & 7, idx = b >> 3;
  const int rb = xcd * 4 + (idx & 3);
  const int ct = idx >> 2;
  const int row0 = rb * 64;
  const int g = lane >> 4, q = (lane & 15) >> 2, p = lane & 3;
  const int arow = wm * 16 + (lane & 15);

  f32x4 acc[3] = {}, st2[2] = {}, cs[3] = {};
  f32x4& st = st2[0];
  float* scr = (float*)(smem + 142 * 1024);

  STAMP(ws, 0, 0);
  if (PF) pf_regions<1>((const char*)(x + (size_t)xcd * 256 * D_), 256 * D_ * 2 / 128, (const char*)wq, D_ * D_ * 2 / 128, idx, 32, wave,
                lane, lds_addr(smem) + 143360);
  if (ct < 7) {
    gemm64<10, K1_RING, true>(smem, x + (size_t)row0 * D_, D_, wq + ct * 96, D_, ct, wave, lane, acc, st2, cs);
    STAMP(ws, 0, 1);
  } else {
    constexpr int WOFF = 80 * 1024;
    {
      const unsigned vo = (unsigned)((lane >> 3) * D_ + (((lane & 7) ^ (lane >> 3)) << 3)) * 2u;
      const char* ab = (const char*)(x + (size_t)(row0 + wave * 8) * D_);
      const unsigned l0 = lds_addr(smem);
#pragma unroll
      for (int s = 0; s < 10; ++s)
        glds16s(vo, ab + s * 128, l0 + s * 8192 + wave * 1024);
    }
    u32x2 wv_[13];
#pragma unroll
    for (int it = 0; it < 13; ++it) {
      int e = min(it * 512 + tid, 6399);
      int k = e / 10, c4 = e - k * 10;
      const u16* src = (c4 < 5) ? (wk + k * 20 + c4 * 4) : (wv + k * 20 + (c4 - 5) * 4);
      wv_[it] = *(const u32x2*)src;
    }
#pragma unroll
    for (int it = 0; it < 13; ++it) {
      int e = min(it * 512 + tid, 6399);
      int k = e / 10, c4 = e - k * 10;
      int kb = k >> 3, cb = c4 >> 2;
      int rp = (k & 7) ^ ((kb & 1) << 2);
      *(LDS_AS u32x2*)(smem + WOFF + (kb * 3 + cb) * 256 + rp * 32 + (c4 & 3) * 8) = wv_[it];
    }
    wait_vm<0>();
    __syncthreads();
    if (wn == 0 || true) {
      const int boff = g * 3 * 256 + q * 32 + p * 8;
      const int b0 = WOFF + boff + (g & 1) * 128, b1 = WOFF + boff + (1 - (g & 1)) * 128;
#pragma unroll
      for (int kstep = 0; kstep < 20; ++kstep) {
        int s = kstep >> 1, ks = kstep & 1;
        int a_off = s * 8192 + arow * 128 + (((ks * 4 + g) ^ (arow & 7)) << 4);
        bf16x8 a = lds_b128(smem + a_off);
        if (wn == 0) {
#pragma unroll
          for (int j = 0; j < 3; ++j) {
            bf16x8 w = lds_tr_pair(smem + b0 + kstep * 4 * 3 * 256 + j * 256, smem + b1 + kstep * 4 * 3 * 256 + j * 256);
            acc[j] = mfma16(w, a, acc[j]);
          }
          st = mfma16(ones8(), a, st);
        } else {
          st = mfma16(a, a, st);
          if ((kstep & 3) == wm) {
#pragma unroll
            for (int j = 0; j < 3; ++j) {
              bf16x8 w = lds_tr_pair(smem + b0 + kstep * 4 * 3 * 256 + j * 256, smem + b1 + kstep * 4 * 3 * 256 + j * 256);
              cs[j] = mfma16(w, ones8(), cs[j]);
            }
          }
        }
      }
    }
  }
  if (ct < 7) {
    const int kh = wave & 1, r32 = (wave & 2) * 16;
#pragma unroll
    for (int mi = 0; mi < 2; ++mi) {
      if (wn == 0) {
        if (g == 0) scr[kh * 64 + r32 + mi * 16 + lane] = st2[mi][0];
      } else {
        if (g == q) scr[128 + kh * 64 + r32 + mi * 16 + (lane & 15)] = st2[mi][p == 0 ? 0 : p == 1 ? 1 : p == 2 ? 2 : 3];
      }
    }
  } else if (wn == 0) {
    if (g == 0) { scr[wm * 16 + lane] = st[0]; scr[64 + wm * 16 + lane] = 0.f; }
  } else {
    if (g == q) {
      scr[128 + wm * 16 + (lane & 15)] = st[p == 0 ? 0 : p == 1 ? 1 : p == 2 ? 2 : 3];
      scr[192 + wm * 16 + (lane & 15)] = 0.f;
    }
  }
  if ((lane & 15) == 0) {
    if (ct < 7) {
#pragma unroll
      for (int j = 0; j < 3; ++j) *(LDS_AS f32x4*)(&scr[256 + wm * 96 + acc_col(ct, wn, j) + g * 4]) = cs[j];
    } else if (wn == 1) {
#pragma unroll
      for (int j = 0; j < 3; ++j) *(LDS_AS f32x4*)(&scr[256 + wm * 96 + j * 16 + g * 4]) = cs[j];
    }
  }
  __syncthreads();
  const float rsum = scr[arow] + scr[64 + arow], rsq = scr[128 + arow] + scr[192 + arow];
  const float mean = rsum * (1.0f / D_);
  const float var = fmaxf(rsq * (1.0f / D_) - mean * mean, 0.0f);
  const float rstd = rsqrtf(var + LN_EPS);
  const int row = row0 + arow;
  if (ct < 7) {
    u16* Q = (u16*)(ws + WS_Q);
    const int n0 = ct * 96;
#pragma unroll
    for (int j = 0; j < 3; ++j) {
      if (ct == 6 && j == 2) continue;
      int nl = acc_col(ct, wn, j) + g * 4;
      f32x4 c = *(LDS_AS f32x4*)&scr[256 + nl];
      c += *(LDS_AS f32x4*)&scr[256 + 96 + nl];
      c += *(LDS_AS f32x4*)&scr[256 + 192 + nl];
      c += *(LDS_AS f32x4*)&scr[256 + 288 + nl];
      float v[4];
#pragma unroll
      for (int r = 0; r < 4; ++r) v[r] = (acc[j][r] - mean * c[r]) * (rstd * QSCALE);
      u32x2 o = {pk_bf16(v[0], v[1]), pk_bf16(v[2], v[3])};
      st_out(Q + (size_t)row * D_ + n0 + nl, o);
    }
  } else if (wn == 0) {
    char* kv = ws + WS_KV + (size_t)(row >> 5) * KVT;
    const int kk = row & 31;
    float ksq = 0.f;
#pragma unroll
    for (int j = 0; j < 3; ++j) {
      int nl = j * 16 + g * 4;
      if (nl >= 40) continue;
      f32x4 c = *(LDS_AS f32x4*)&scr[256 + nl];
      c += *(LDS_AS f32x4*)&scr[256 + 96 + nl];
      c += *(LDS_AS f32x4*)&scr[256 + 192 + nl];
      c += *(LDS_AS f32x4*)&scr[256 + 288 + nl];
      float v[4];
#pragma unroll
      for (int r = 0; r < 4; ++r) v[r] = (acc[j][r] - mean * c[r]) * rstd;
      u32x2 o = {pk_bf16(v[0], v[1]), pk_bf16(v[2], v[3])};
      if (nl < 20) {
        float a0 = bf_lo(o[0]), a1 = bf_hi(o[0]), a2 = bf_lo(o[1]), a3 = bf_hi(o[1]);
        ksq += a0 * a0 + a1 * a1 + a2 * a2 + a3 * a3;
        if (nl < 16) {
          *(u32x2*)(kv + ((nl >> 3) * 32 + kk) * 16 + (nl & 7) * 2) = o;
        } else {
          *(u32x2*)(kv + 1024 + kk * 8) = o;
          u32x2 cst = {0x3f803f80u, 0x00003f80u};
          *(u32x2*)(kv + 1024 + 256 + kk * 8) = cst;
        }
      } else {
        int d = nl - 20;
        int s = kk >> 4, k16 = kk & 15;
        int h = (k16 >> 2) & 1, jj = ((k16 >> 3) << 2) | (k16 & 3);
        u16* vt = (u16*)(kv + 1536) + ((s * 2 + h) * 21 + d) * 8 + jj;
        vt[0] = (u16)(o[0] & 0xffff);
        vt[8] = (u16)(o[0] >> 16);
        vt[16] = (u16)(o[1] & 0xffff);
        vt[24] = (u16)(o[1] >> 16);
        if (d == 16) vt[32] = (u16)0x3f80;
      }
    }
    ksq += __shfl_xor(ksq, 16);
    ksq += __shfl_xor(ksq, 32);
#pragma unroll
    for (int o = 1; o < 16; o <<= 1) ksq = fmaxf(ksq, __shfl_xor(ksq, o));
    if (lane == 0) ((float*)(ws + WS_KMAX))[rb * 4 + wm] = ksq;
  }
  STAMP(ws, 0, 2);
}

constexpr int AT_STAGE = 8 * KVT;
constexpr int AT_LDS = 4 * AT_STAGE;

__device__ __forceinline__ void split3(float m, unsigned& w0, unsigned& w1) {
  float a = -m;
  unsigned ua = __builtin_bit_cast(unsigned, a) & 0xffff0000u;
  float r1 = a - __builtin_bit_cast(float, ua);
  unsigned ub = __builtin_bit_cast(unsigned, r1) & 0xffff0000u;
  float r2 = r1 - __builtin_bit_cast(float, ub);
  unsigned uc = __builtin_bit_cast(unsigned, r2) & 0xffff0000u;
  w0 = (ua >> 16) | ub;
  w1 = uc >> 16;
}

struct KFrag { bf16x8 k0; s16x4 k1; };
struct VFrag { bf16x8 v0, v1; };
__device__ __forceinline__ void ld_k(KFrag& f, const char* kt, int lane) {
  f.k0 = lds_b128(kt + lane * 16);
  f.k1 = *(const LDS_AS s16x4*)(kt + 1024 + lane * 8);
}
__device__ __forceinline__ void ld_v(VFrag& f, const char* kt, int voff) {
  f.v0 = lds_b128(kt + 1536 + voff);
  f.v1 = lds_b128(kt + 1536 + 672 + voff);
}
__device__ __forceinline__ f32x16 qk(const KFrag& k, bf16x8 q0, s16x4 q1) {
  f32x16 z = {};
  f32x16 s = __builtin_amdgcn_mfma_f32_32x32x16_bf16(k.k0, q0, z, 0, 0, 0);
  return __builtin_amdgcn_mfma_f32_32x32x8bf16_1k(k.k1, q1, s, 0, 0, 0);
}
#define SB() __builtin_amdgcn_sched_barrier(0)
#define EX(i) __builtin_amdgcn_exp2f(sc[i])
__device__ __forceinline__ void attn_unit(const f32x16& sc, const VFrag& v, f32x16& o, f32x16& sn, const KFrag& k, bf16x8 q0,
                                          s16x4 q1) {
  u32x4 p0, p1;
  f32x16 z = {};
  float e0 = EX(0), e1 = EX(1), e2 = EX(2), e3 = EX(3);
  SB();
  sn = __builtin_amdgcn_mfma_f32_32x32x16_bf16(k.k0, q0, z, 0, 0, 0);
  SB();
  p0[0] = pk_bf16(e0, e1);
  p0[1] = pk_bf16(e2, e3);
  p0[2] = pk_bf16(EX(4), EX(5));
  p0[3] = pk_bf16(EX(6), EX(7));
  SB();
  o = __builtin_amdgcn_mfma_f32_32x32x16_bf16(v.v0, __builtin_bit_cast(bf16x8, p0), o, 0, 0, 0);
  SB();
  e0 = EX(8), e1 = EX(9), e2 = EX(10), e3 = EX(11);
  SB();
  sn = __builtin_amdgcn_mfma_f32_32x32x8bf16_1k(k.k1, q1, sn, 0, 0, 0);
  SB();
  p1[0] = pk_bf16(e0, e1);
  p1[1] = pk_bf16(e2, e3);
  p1[2] = pk_bf16(EX(12), EX(13));
  p1[3] = pk_bf16(EX(14), EX(15));
  SB();
  o = __builtin_amdgcn_mfma_f32_32x32x16_bf16(v.v1, __builtin_bit_cast(bf16x8, p1), o, 0, 0, 0);
  SB();
}

__global__ __launch_bounds__(512, 2) void k2_attn(char* __restrict__ ws) {
  extern __shared__ __attribute__((aligned(16))) char smem[];
  const int tid = threadIdx.x, lane = tid & 63;
  const int wave = __builtin_amdgcn_readfirstlane(tid >> 6);
  const int grp = wave >> 2, wq = wave & 3;
  const int r = lane & 31, h = lane >> 5;
  const char* kvg = ws + WS_KV;
  const int pos0 = blockIdx.x * 8 + wq * 2;

  unsigned kvo[6];
#pragma unroll
  for (int it = 0; it < 6; ++it) {
    int c = it * 512 + wave * 64 + lane;
    int gg = c >= 1440;
    kvo[it] = (unsigned)(gg * 4 * AT_STAGE + (c - gg * 1440) * 16);
  }
  const unsigned l0 = lds_addr(smem);
  auto issue = [&](int st, int buf) {
#pragma unroll
    for (int it = 0; it < 6; ++it) {
      if (it < 5 || wave < 5) glds16s(kvo[it], kvg + (size_t)st * AT_STAGE, l0 + buf * 2 * AT_STAGE + (it * 512 + wave * 64) * 16);
    }
  };
  STAMP(ws, 1, 0);
  issue(0, 0);

  issue(1, 1);

  float kmax2;
  {
    const float* km = (const float*)(ws + WS_KMAX);
    float m = fmaxf(km[lane], km[lane + 64]);
#pragma unroll
    for (int o = 1; o < 64; o <<= 1) m = fmaxf(m, __shfl_xor(m, o));
    kmax2 = m;
  }
  bf16x8 q0[2];
  s16x4 q1[2];
  u32x2 qtail[2];
  float mshift[2];
  const u16* Qs = (const u16*)(ws + WS_Q);
#pragma unroll
  for (int t = 0; t < 2; ++t) {
    const u16* qp = Qs + (size_t)(pos0 + t) * D_ + r * 20;
    u32x2 a = *(const u32x2*)(qp + 8 * h), b2 = *(const u32x2*)(qp + 8 * h + 4);
    u32x4 qq = {a[0], a[1], b2[0], b2[1]};
    q0[t] = __builtin_bit_cast(bf16x8, qq);
    u32x2 tl = *(const u32x2*)(qp + 16);
    qtail[t] = tl;
    float ss = 0.f;
#pragma unroll
    for (int i = 0; i < 4; ++i) { float x0 = bf_lo(qq[i]), x1 = bf_hi(qq[i]); ss += x0 * x0 + x1 * x1; }
    if (h == 0) {
#pragma unroll
      for (int i = 0; i < 2; ++i) { float x0 = bf_lo(tl[i]), x1 = bf_hi(tl[i]); ss += x0 * x0 + x1 * x1; }
    }
    ss += __shfl_xor(ss, 32);
    mshift[t] = sqrtf(ss * kmax2) * 1.0001f;
  }

  const int voff = (h * 21 + (r < 21 ? r : 20)) * 16;
  f32x16 o[2];
  float* xch = (float*)smem;
  int* flag = (int*)(smem + AT_LDS + 512);

  for (int attempt = 0;; ++attempt) {
#pragma unroll
    for (int t = 0; t < 2; ++t) {
      unsigned w0, w1;
      split3(mshift[t], w0, w1);
      u32x2 f = (h == 0) ? qtail[t] : u32x2{w0, w1};
      q1[t] = __builtin_bit_cast(s16x4, f);
#pragma unroll
      for (int i = 0; i < 16; ++i) o[t][i] = 0.f;
    }
    if (attempt) { issue(0, 0); issue(1, 1); }
    STAMP(ws, 1, 1);
    wait_vm<0>();
    bar();
    KFrag kc, kn;
    VFrag vc, vn;
    {
      const char* kb = smem + grp * AT_STAGE;
      ld_k(kc, kb, lane);
      ld_v(vc, kb, voff);
    }
    f32x16 sa = qk(kc, q0[0], q1[0]), sb;
#pragma unroll 1
    for (int st = 0; st < 4; ++st) {
      const char* kb = smem + (st & 1) * 2 * AT_STAGE + grp * AT_STAGE;
      const char* kbn = smem + ((st + 1) & 1) * 2 * AT_STAGE + grp * AT_STAGE;
#pragma unroll
      for (int kt = 0; kt < 8; ++kt) {
        if (kt == 7) {
          wait_vm<0>();
          lds_fence();
          bar();
          if (st < 2) issue(st + 2, st & 1);
        }
        const char* nx = kt < 7 ? kb + (kt + 1) * KVT : kbn;
        ld_k(kn, nx, lane);
        ld_v(vn, nx, voff);
        SB();
        attn_unit(sa, vc, o[0], sb, kc, q0[1], q1[1]);
        attn_unit(sb, vc, o[1], sa, kn, q0[0], q1[0]);
        kc = kn;
        vc = vn;
      }
    }
    __syncthreads();
    STAMP(ws, 1, 2);
    if (grp == 1) {
#pragma unroll
      for (int t = 0; t < 2; ++t)
#pragma unroll
        for (int i = 0; i < 12; ++i) xch[((wq * 2 + t) * 12 + i) * 64 + lane] = o[t][i];
    }
    if (tid == 0) *flag = 0;
    __syncthreads();
    bool bad = false;
    float linv[2];
    if (grp == 0) {
#pragma unroll
      for (int t = 0; t < 2; ++t) {
#pragma unroll
        for (int i = 0; i < 12; ++i) o[t][i] += xch[((wq * 2 + t) * 12 + i) * 64 + lane];
        float l = __shfl(o[t][8], 32 + r);
        bad |= !(l >= 5.421010862427522e-20f);
        linv[t] = 1.0f / l;
      }
      if (attempt == 0 && bad) *flag = 1;
    }
    __syncthreads();
    if (attempt == 1 || *flag == 0) {
      if (grp == 0) {
        u16* O = (u16*)(ws + WS_O);
#pragma unroll
        for (int t = 0; t < 2; ++t) {
          u16* op = O + (size_t)(pos0 + t) * D_ + r * 20 + 4 * h;
#pragma unroll
          for (int gq = 0; gq < 3; ++gq) {
            if (gq == 2 && h == 1) continue;
            u32x2 v = {pk_bf16(o[t][4 * gq] * linv[t], o[t][4 * gq + 1] * linv[t]),
                       pk_bf16(o[t][4 * gq + 2] * linv[t], o[t][4 * gq + 3] * linv[t])};
            st_out(op + 8 * gq, v);
          }
        }
      }
      break;
    }
    __syncthreads();
#pragma unroll 1
    for (int t = 0; t < 2; ++t) {
      const u16* qp = Qs + (size_t)(pos0 + t) * D_ + r * 20;
      float qf[20];
#pragma unroll
      for (int i = 0; i < 10; ++i) { unsigned u = *(const unsigned*)(qp + 2 * i); qf[2 * i] = bf_lo(u); qf[2 * i + 1] = bf_hi(u); }
      float mx = -INFINITY;
#pragma unroll 1
      for (int kk = 0; kk < 512; ++kk) {
        int key = grp * 1024 + h * 512 + kk;
        const char* t0 = kvg + (size_t)(key >> 5) * KVT;
        int kr = key & 31;
        u32x4 a = *(const u32x4*)(t0 + kr * 16), b2 = *(const u32x4*)(t0 + 512 + kr * 16);
        u32x2 c = *(const u32x2*)(t0 + 1024 + kr * 8);
        float sacc = 0.f;
#pragma unroll
        for (int i = 0; i < 4; ++i) {
          sacc += qf[2 * i] * bf_lo(a[i]) + qf[2 * i + 1] * bf_hi(a[i]);
          sacc += qf[8 + 2 * i] * bf_lo(b2[i]) + qf[9 + 2 * i] * bf_hi(b2[i]);
        }
        sacc += qf[16] * bf_lo(c[0]) + qf[17] * bf_hi(c[0]) + qf[18] * bf_lo(c[1]) + qf[19] * bf_hi(c[1]);
        mx = fmaxf(mx, sacc);
      }
      mx = fmaxf(mx, __shfl_xor(mx, 32));
      if (h == 0) xch[(grp * 8 + wq * 2 + t) * 32 + r] = mx;
    }
    __syncthreads();
#pragma unroll
    for (int t = 0; t < 2; ++t) mshift[t] = fmaxf(xch[(wq * 2 + t) * 32 + r], xch[(8 + wq * 2 + t) * 32 + r]);
    __syncthreads();
  }
  STAMP(ws, 1, 3);
}

constexpr int G2_RING = 7;
constexpr int G2_LDS = G2_RING * ST64 + 256;

template <int K, bool STATS>
__global__ __launch_bounds__(512, 2) void k_gemm96(const u16* __restrict__ A, const u16* __restrict__ W,
                                                   const u16* __restrict__ res, u16* __restrict__ out,
                                                   float* __restrict__ stats, char* __restrict__ ws) {
  extern __shared__ __attribute__((aligned(16))) char smem[];
  const int tid = threadIdx.x, lane = tid & 63;
  const int wave = __builtin_amdgcn_readfirstlane(tid >> 6);
  const int wm = wave & 3, wn = wave >> 2;
  const int b = blockIdx.x;
  const int xcd = b & 7, idx = b >> 3;
  const int rb = xcd * 4 + (idx & 3), ct = idx >> 2;
  const int row0 = rb * 64, n0 = ct * 96;
  const int g = lane >> 4;
  const int arow = wm * 16 + (lane & 15);
  const int row = row0 + arow;
  const int nj = (ct == 6) ? 2 : 3;
  STAMP(ws, STATS ? 2 : 4, 0);
  if (PF) pf_regions<(K > 640 ? 2 : 1)>((const char*)(A + (size_t)xcd * 256 * K), 256 * K * 2 / 128, (const char*)W, K * D_ * 2 / 128, idx,
                                28, wave, lane, lds_addr(smem) + G2_RING * ST64);

  u32x2 rv[3];
#pragma unroll
  for (int j = 0; j < 3; ++j) {
    int c = (j < nj) ? acc_col(ct, wn, j) : 0;
    rv[j] = *(const u32x2*)(res + (size_t)row * D_ + n0 + c + g * 4);
  }
  f32x4 acc[3] = {}, st[2] = {}, cs[3] = {};
  gemm64<K / 64, G2_RING, false>(smem, A + (size_t)row0 * K, K, W + n0, D_, ct, wave, lane, acc, st, cs);
  STAMP(ws, STATS ? 2 : 4, 1);

  float sum = 0.f;
  float hv[3][4];
#pragma unroll
  for (int j = 0; j < 3; ++j) {
    if (j < nj) {
      float v0 = acc[j][0] + bf_lo(rv[j][0]), v1 = acc[j][1] + bf_hi(rv[j][0]);
      float v2 = acc[j][2] + bf_lo(rv[j][1]), v3 = acc[j][3] + bf_hi(rv[j][1]);
      u32x2 o = {pk_bf16(v0, v1), pk_bf16(v2, v3)};
      st_out(out + (size_t)row * D_ + n0 + acc_col(ct, wn, j) + g * 4, o);
      if (STATS) {
        hv[j][0] = bf_lo(o[0]); hv[j][1] = bf_hi(o[0]); hv[j][2] = bf_lo(o[1]); hv[j][3] = bf_hi(o[1]);
        sum += hv[j][0] + hv[j][1] + hv[j][2] + hv[j][3];
      }
    } else if (STATS) {
      hv[j][0] = hv[j][1] = hv[j][2] = hv[j][3] = 0.f;
    }
  }
  if (STATS) {
    float* scr = (float*)smem;
    sum += __shfl_xor(sum, 16);
    sum += __shfl_xor(sum, 32);
    __syncthreads();
    if (g == 0) scr[wn * 64 + arow] = sum;
    __syncthreads();
    const float tot = scr[arow] + scr[64 + arow];
    const float mu = tot * ((ct == 6) ? (1.0f / 64.0f) : (1.0f / 96.0f));
    float m2 = 0.f;
#pragma unroll
    for (int j = 0; j < 3; ++j) {
      if (j < nj) {
#pragma unroll
        for (int r = 0; r < 4; ++r) { float d = hv[j][r] - mu; m2 += d * d; }
      }
    }
    m2 += __shfl_xor(m2, 16);
    m2 += __shfl_xor(m2, 32);
    if (g == 0) scr[128 + wn * 64 + arow] = m2;
    __syncthreads();
    if (wn == 0 && g == 0) {
      f32x2 o = {tot, scr[128 + arow] + scr[192 + arow]};
      *(f32x2*)(stats + ((size_t)row * 8 + ct) * 2) = o;
    }
  }
  STAMP(ws, STATS ? 2 : 4, 2);
}

constexpr int G3_RING = 4;
constexpr int G3_LDS = G3_RING * 40 * 1024;

__global__ __launch_bounds__(512, 2) void k4_ffn_up(const u16* __restrict__ Hh, const u16* __restrict__ wg,
                                                    const u16* __restrict__ wu, const float* __restrict__ stats,
                                                    u16* __restrict__ act,
                                                    char* __restrict__ ws) {
  extern __shared__ __attribute__((aligned(16))) char smem[];
  constexpr int ST = 40 * 1024;
  const int tid = threadIdx.x, lane = tid & 63;
  const int wave = __builtin_amdgcn_readfirstlane(tid >> 6);
  const int wm = wave & 3, wn = wave >> 2;
  const int b = blockIdx.x;
  const int xcd = b & 7, idx = b >> 3;
  const int rb = (xcd & 1) * 8 + (idx & 7);
  const int ct = (xcd >> 1) * 4 + (idx >> 3);
  const int row0 = rb * 128, n0 = ct * 96;
  const int g = lane >> 4;
  STAMP(ws, 3, 0);
  if (PF) {
    const char* hb = (const char*)(Hh + (size_t)(xcd & 1) * 1024 * D_);
    const int wc0 = (xcd >> 1) * 384;
#pragma unroll
    for (int t = 0; t < 2; ++t) {
      int i0 = (wave + t * 8) * 64;
      if (i0 < 560) {
        int l = min(idx * 560 + i0 + lane, 17919);
        int lw = l - 10240;
        int m = lw >= 3840;
        lw -= m * 3840;
        int k = lw / 6, c = lw - k * 6;
        const char* wp = (const char*)((m ? wu : wg) + (size_t)k * F_ + wc0) + c * 128;
        pf_line(l < 10240 ? hb + (size_t)l * 128 : wp, lds_addr(smem) + 3 * ST + ST - 256);
      }
    }
  }

  const f32x4* sp = (const f32x4*)(stats + (size_t)(row0 + (tid & 127)) * 16);
  const f32x4 s0 = sp[0], s1 = sp[1], s2 = sp[2];
  const f32x2 s3 = *(const f32x2*)(sp + 3);
  const int c64 = (ct & 1) ? 32 : 0, c32 = (ct & 1) ? 0 : 64;
  const unsigned va = voff_a(wave * 16, lane, D_);
  const unsigned v64 = voff_g64(wave, lane, F_, c64), v32 = voff_g32(wave & 3, lane, F_, c32);
  const char* abase = (const char*)(Hh + (size_t)row0 * D_);
  const char* gbase = (const char*)(wg + n0);
  const char* ubase = (const char*)(wu + n0);
  const unsigned l0 = lds_addr(smem);
  auto issue = [&](int s, int slot) {
    const unsigned d = l0 + slot * ST;
    const char* gw = gbase + (size_t)s * 64 * F_ * 2;
    const char* uw = ubase + (size_t)s * 64 * F_ * 2;
    glds16s(va, abase + s * 128, d + wave * 2048);
    glds16s(va, abase + s * 128 + 8 * D_ * 2, d + wave * 2048 + 1024);
    glds16s(v64, gw, d + 16384 + wave * 1024);
    glds16s(v64, uw, d + 24576 + wave * 1024);
    glds16s(v32, wave < 4 ? gw : uw, d + 32768 + wave * 1024);
  };
#pragma unroll
  for (int s = 0; s < G3_RING - 1; ++s) issue(s, s);

  const int wo0 = 16384 + roff_g64(wn * 2, lane), wo1 = 16384 + roff_g64(wn * 2 + 1, lane), wo2 = 32768 + roff_g32(wn, lane);
  const int ar0 = wm * 32 + (lane & 15);
  f32x4 ag[2][3] = {}, au[2][3] = {};
  struct Frag { bf16x8 a[2], g[3], u[3]; };
  const int ao0 = ar0 * 128 + ((g ^ (ar0 & 7)) << 4), ao1 = ar0 * 128 + (((4 + g) ^ (ar0 & 7)) << 4);
  auto rd = [&](Frag& f, const char* buf, int ks) {
    f.a[0] = lds_b128(buf + (ks ? ao1 : ao0));
    f.g[0] = rd_g64(buf + wo0, ks);
    f.u[0] = rd_g64(buf + wo0 + 8192, ks);
    f.a[1] = lds_b128(buf + (ks ? ao1 : ao0) + 2048);
    f.g[1] = rd_g64(buf + wo1, ks);
    f.u[1] = rd_g64(buf + wo1 + 8192, ks);
    f.g[2] = rd_g32(buf + wo2, ks);
    f.u[2] = rd_g32(buf + wo2 + 4096, ks);
  };
  f32x4 cg[3] = {}, cu[3] = {};
  auto mm = [&](const Frag& f, int step) {
#pragma unroll
    for (int j = 0; j < 3; ++j)
#pragma unroll
      for (int mi = 0; mi < 2; ++mi) {
        ag[mi][j] = mfma16(f.g[j], f.a[mi], ag[mi][j]);
        au[mi][j] = mfma16(f.u[j], f.a[mi], au[mi][j]);
      }
    if ((step & 3) == wm) {
#pragma unroll
      for (int j = 0; j < 3; ++j) {
        cg[j] = mfma16(f.g[j], ones8(), cg[j]);
        cu[j] = mfma16(f.u[j], ones8(), cu[j]);
      }
    }
  };
  Frag f0, f1;
  wait_vm<10>();
  bar();
  STAMP2(3, 0);
  rd(f0, smem, 0);
#pragma unroll
  for (int s = 0; s < 10; ++s) {
    const char* buf = smem + (s % G3_RING) * ST;
    rd(f1, buf, 1);
    __builtin_amdgcn_sched_barrier(0);
    mm(f0, s * 2);
    __builtin_amdgcn_sched_barrier(0);
    if (s < 9) {
      if (s < 8) wait_vm<5>();
      else wait_vm<0>();
      bar();
      STAMP2(3, s + 1);
      if (s + 3 < 10) issue(s + 3, (s + 3) % G3_RING);
      rd(f0, smem + ((s + 1) % G3_RING) * ST, 0);
      __builtin_amdgcn_sched_barrier(0);
    }
    mm(f1, s * 2 + 1);
    __builtin_amdgcn_sched_barrier(0);
  }
  bar();
  STAMP(ws, 3, 1);
  float* scr = (float*)smem;
  float mean_r = 0.f, rstd_r = 0.f;
  if (tid < 128) {
    float tot = s0[0] + s0[2] + s1[0] + s1[2] + s2[0] + s2[2] + s3[0];
    float mean = tot * (1.0f / D_);
    float m2 = s0[1] + s0[3] + s1[1] + s1[3] + s2[1] + s2[3] + s3[1];
    float c = 0.f, d;
    d = s0[0] * (1.f / 96.f) - mean; c += d * d; d = s0[2] * (1.f / 96.f) - mean; c += d * d;
    d = s1[0] * (1.f / 96.f) - mean; c += d * d; d = s1[2] * (1.f / 96.f) - mean; c += d * d;
    d = s2[0] * (1.f / 96.f) - mean; c += d * d; d = s2[2] * (1.f / 96.f) - mean; c += d * d;
    d = s3[0] * (1.f / 64.f) - mean;
    float var = (m2 + 96.f * c + 64.f * d * d) * (1.0f / D_);
    mean_r = mean;
    rstd_r = rsqrtf(var + LN_EPS);
  }

  if (tid < 128) { scr[tid * 2] = mean_r; scr[tid * 2 + 1] = rstd_r; }
  if ((lane & 15) == 0) {
#pragma unroll
    for (int j = 0; j < 3; ++j) {
      *(LDS_AS f32x4*)&scr[256 + wm * 192 + acc_col(ct, wn, j) + g * 4] = cg[j];
      *(LDS_AS f32x4*)&scr[256 + wm * 192 + 96 + acc_col(ct, wn, j) + g * 4] = cu[j];
    }
  }
  __syncthreads();
#pragma unroll
  for (int j = 0; j < 3; ++j) {
    const int nl = 256 + acc_col(ct, wn, j) + g * 4;
    cg[j] = *(LDS_AS f32x4*)&scr[nl] + *(LDS_AS f32x4*)&scr[nl + 192] + *(LDS_AS f32x4*)&scr[nl + 384] + *(LDS_AS f32x4*)&scr[nl + 576];
    cu[j] = *(LDS_AS f32x4*)&scr[nl + 96] + *(LDS_AS f32x4*)&scr[nl + 288] + *(LDS_AS f32x4*)&scr[nl + 480] + *(LDS_AS f32x4*)&scr[nl + 672];
  }
#pragma unroll
  for (int mi = 0; mi < 2; ++mi) {
    const int ar = ar0 + mi * 16;
    const f32x2 ms = *(LDS_AS f32x2*)&scr[ar * 2];
    const float rstd = ms[1], nm = -ms[0] * ms[1];
#pragma unroll
    for (int j = 0; j < 3; ++j) {
      const int n = n0 + acc_col(ct, wn, j) + g * 4;
      float o[4];
#pragma unroll
      for (int r = 0; r < 4; ++r) {
        float gv = ag[mi][j][r] * rstd + nm * cg[j][r];
        float uv = au[mi][j][r] * rstd + nm * cu[j][r];
        float t = gv * gv * (0.044715f * 1.5957691216057308f * 1.4426950408889634f) + (1.5957691216057308f * 1.4426950408889634f);
        float e = __builtin_amdgcn_exp2f(-gv * t);
        o[r] = gv * uv * __builtin_amdgcn_rcpf(1.0f + e);
      }
      u32x2 v = {pk_bf16(o[0], o[1]), pk_bf16(o[2], o[3])};
      st_out(act + (size_t)(row0 + ar) * F_ + n, v);
    }
  }
  STAMP(ws, 3, 2);
}

extern "C" void kernel_launch(void* const* d_in, const int* in_sizes, int n_in, void* d_out, int out_size,
                              void* d_ws, size_t ws_size, hipStream_t stream) {
  const u16* x = (const u16*)d_in[0];
  const u16* wq = (const u16*)d_in[1];
  const u16* wk = (const u16*)d_in[2];
  const u16* wv = (const u16*)d_in[3];
  const u16* wo = (const u16*)d_in[4];
  const u16* wg = (const u16*)d_in[6];
  const u16* wu = (const u16*)d_in[7];
  const u16* wd = (const u16*)d_in[8];
  char* ws = (char*)d_ws;
  u16* out = (u16*)d_out;

  (void)hipFuncSetAttribute((const void*)k1_qkv, hipFuncAttributeMaxDynamicSharedMemorySize, K1_LDS);
  (void)hipFuncSetAttribute((const void*)k2_attn, hipFuncAttributeMaxDynamicSharedMemorySize, AT_LDS + 1024);
  (void)hipFuncSetAttribute((const void*)k_gemm96<640, true>, hipFuncAttributeMaxDynamicSharedMemorySize, G2_LDS);
  (void)hipFuncSetAttribute((const void*)k_gemm96<1536, false>, hipFuncAttributeMaxDynamicSharedMemorySize, G2_LDS);
  (void)hipFuncSetAttribute((const void*)k4_ffn_up, hipFuncAttributeMaxDynamicSharedMemorySize, G3_LDS);

  k1_qkv<<<256, 512, K1_LDS, stream>>>(x, wq, wk, wv, ws);
  k2_attn<<<256, 512, AT_LDS + 1024, stream>>>(ws);
  k_gemm96<640, true><<<224, 512, G2_LDS, stream>>>((const u16*)(ws + WS_O), wo, x, (u16*)(ws + WS_H),
                                                    (float*)(ws + WS_STAT), ws);
  k4_ffn_up<<<256, 512, G3_LDS, stream>>>((const u16*)(ws + WS_H), wg, wu, (const float*)(ws + WS_STAT),
                                          (u16*)(ws + WS_ACT), ws);
  k_gemm96<1536, false><<<224, 512, G2_LDS, stream>>>((const u16*)(ws + WS_ACT), wd, (const u16*)(ws + WS_H), out,
                                                      (float*)(ws + WS_STAT), ws);
}
```

```cpp
#include <hip/hip_runtime.h>
#include <hip/hip_bf16.h>

typedef __attribute__((ext_vector_type(8))) __bf16 bf16x8;
typedef __attribute__((ext_vector_type(4))) __bf16 bf16x4;
typedef __attribute__((ext_vector_type(2))) __bf16 bf16x2;
typedef __attribute__((ext_vector_type(4))) short s16x4;
typedef __attribute__((ext_vector_type(16))) float f32x16;
typedef __attribute__((ext_vector_type(4))) float f32x4;
typedef __attribute__((ext_vector_type(2))) float f32x2;
typedef __attribute__((ext_vector_type(4))) unsigned u32x4;
typedef __attribute__((ext_vector_type(2))) unsigned u32x2;
typedef unsigned short u16;

#define LDS_AS __attribute__((address_space(3)))
#define GLB_AS __attribute__((address_space(1)))

#define PF 0
constexpr int S_ = 2048, D_ = 640, F_ = 1536;
constexpr float LN_EPS = 1e-5f;
constexpr float QSCALE = 0.22360679774997896f * 1.4426950408889634f;

constexpr int KVT = 2880;
constexpr int KV_BYTES = 64 * KVT;

constexpr size_t WS_Q = 0;
constexpr size_t WS_KV = WS_Q + (size_t)S_ * D_ * 2;
constexpr size_t WS_KMAX = WS_KV + KV_BYTES;
constexpr size_t WS_O = WS_KMAX + 512;
constexpr size_t WS_H = WS_O + (size_t)S_ * D_ * 2;
constexpr size_t WS_STAT = WS_H + (size_t)S_ * D_ * 2;
constexpr size_t WS_CS = WS_STAT + (size_t)S_ * 8 * 2 * 4;
constexpr size_t WS_ACT = WS_CS + 3072 * 4;

__device__ __forceinline__ unsigned pk_bf16(float a, float b) {
  f32x2 f = {a, b};
  return __builtin_bit_cast(unsigned, __builtin_convertvector(f, bf16x2));
}
__device__ __forceinline__ float bf_lo(unsigned u) { return __builtin_bit_cast(float, u << 16); }
__device__ __forceinline__ float bf_hi(unsigned u) { return __builtin_bit_cast(float, u & 0xffff0000u); }

#define STAMP(ws_, kid, i)
#define STAMP2(kid, i)
template <int N> __device__ __forceinline__ void wait_vm() { asm volatile("s_waitcnt vmcnt(%0)" ::"i"(N) : "memory"); }
__device__ __forceinline__ void bar() { __builtin_amdgcn_s_barrier(); }
__device__ __forceinline__ void glds16s(unsigned voff, const void* sbase, unsigned lds) {
  asm volatile("s_mov_b32 m0, %2\n\ts_nop 3\n\tglobal_load_lds_dwordx4 %0, %1" ::"v"(voff), "s"(sbase), "s"(lds)
               : "memory");
}
__device__ __forceinline__ void pf_line(const char* addr, unsigned lds_dump) {
  asm volatile("s_mov_b32 m0, %1\n\ts_nop 3\n\tglobal_load_lds_dword %0, off" ::"v"(addr), "s"(lds_dump) : "memory");
}
template <int NI>
__device__ __forceinline__ void pf_regions(const char* A, int nA, const char* B, int nB, int part, int nparts, int wave,
                                           int lane, unsigned lds_dump) {
  const int per = (nA + nB + nparts - 1) / nparts;
#pragma unroll
  for (int t = 0; t < NI; ++t) {
    int i0 = (wave + t * 8) * 64;
    if (i0 < per) {
      int l = part * per + i0 + lane;
      l = min(l, nA + nB - 1);
      pf_line(l < nA ? A + (size_t)l * 128 : B + (size_t)(l - nA) * 128, lds_dump);
    }
  }
}
#define WT 0
__device__ __forceinline__ void st_out(void* p, u32x2 v) {
  *(u32x2*)p = v;
}
__device__ __forceinline__ unsigned lds_addr(const char* p) { return (unsigned)(size_t)(const LDS_AS char*)p; }
__device__ __forceinline__ void lds_fence() { asm volatile("s_waitcnt lgkmcnt(0)" ::: "memory"); }
__device__ __forceinline__ bf16x8 lds_b128(const char* p) { return *(const LDS_AS bf16x8*)(p); }
__device__ __forceinline__ bf16x8 lds_tr_pair(const char* p0, const char* p1) {
  bf16x4 a = __builtin_amdgcn_ds_read_tr16_b64_v4bf16((LDS_AS bf16x4*)p0);
  bf16x4 b = __builtin_amdgcn_ds_read_tr16_b64_v4bf16((LDS_AS bf16x4*)p1);
  return __builtin_shufflevector(a, b, 0, 1, 2, 3, 4, 5, 6, 7);
}
__device__ __forceinline__ f32x4 mfma16(bf16x8 a, bf16x8 b, f32x4 c) {
  return __builtin_amdgcn_mfma_f32_16x16x32_bf16(a, b, c, 0, 0, 0);
}
__device__ __forceinline__ bf16x8 ones8() {
  u32x4 o = {0x3f803f80u, 0x3f803f80u, 0x3f803f80u, 0x3f803f80u};
  return __builtin_bit_cast(bf16x8, o);
}

__device__ __forceinline__ unsigned voff_a(int row, int lane, int lda) {
  return (unsigned)((row + (lane >> 3)) * lda + (((lane & 7) ^ (lane >> 3)) << 3)) * 2u;
}
__device__ __forceinline__ unsigned voff_g64(int kb, int lane, int ldw, int c64) {
  int r = lane >> 3, pos = lane & 7;
  int u = (pos >> 1) ^ (((r >> 1) & 1) | ((kb & 1) << 1));
  return (unsigned)((kb * 8 + r) * ldw + c64 + u * 16 + (pos & 1) * 8) * 2u;
}
__device__ __forceinline__ unsigned voff_g32(int kbp, int lane, int ldw, int c32) {
  int kb = kbp * 2 + (lane >> 5), r = (lane >> 2) & 7, pos = lane & 3;
  int u = (pos >> 1) ^ (kb & 1);
  return (unsigned)((kb * 8 + r) * ldw + c32 + u * 16 + (pos & 1) * 8) * 2u;
}
__device__ __forceinline__ int roff_g64(int u, int lane) {
  int g = lane >> 4, q = (lane & 15) >> 2, p = lane & 3;
  return g * 1024 + q * 128 + ((u ^ (((q >> 1) & 1) | ((g & 1) << 1))) << 5) + p * 8;
}
__device__ __forceinline__ int roff_g32(int v, int lane) {
  int g = lane >> 4, q = (lane & 15) >> 2, p = lane & 3;
  return g * 512 + q * 64 + ((v ^ (g & 1)) << 5) + p * 8;
}
__device__ __forceinline__ bf16x8 rd_g64(const char* p, int ks) { return lds_tr_pair(p + ks * 4096, p + ks * 4096 + 512); }
__device__ __forceinline__ bf16x8 rd_g32(const char* p, int ks) { return lds_tr_pair(p + ks * 2048, p + ks * 2048 + 256); }

constexpr int ST64 = 20 * 1024;

struct Frag64 { bf16x8 a[2], w[3]; };
template <int NS, int RING, int NPW, bool HAS32, bool QKV>
__device__ __forceinline__ void gemm64_loop(char* smem, const char* abase, const char* wbase, int ldw2, unsigned va,
                                            unsigned v64, unsigned v32, int wave, int lane, f32x4 (&out)[3], f32x4 (&st)[2],
                                            f32x4 (&cs)[3]) {
  const int kh = wave & 1, wm2 = (wave >> 1) & 1, wn = wave >> 2, g = lane >> 4;
  const unsigned l0 = lds_addr(smem);
  const int arow = wm2 * 32 + (lane & 15);
  const int ao = arow * 128 + (((kh * 4 + g) ^ (arow & 7)) << 4);
  const int wo0 = 8192 + kh * 4096 + roff_g64(wn * 2, lane), wo1 = 8192 + kh * 4096 + roff_g64(wn * 2 + 1, lane);
  const int wo2 = 16384 + kh * 2048 + roff_g32(wn, lane);
  f32x4 acc[2][3] = {};
  auto issue = [&](int s, int slot) {
    const unsigned d = l0 + slot * ST64 + wave * 1024;
    const char* w = wbase + (size_t)s * 64 * ldw2;
    glds16s(va, abase + s * 128, d);
    glds16s(v64, w, d + 8192);
    if (NPW == 3) glds16s(v32, w, d + 16384);
  };
  auto rd = [&](Frag64& f, int slot) {
    const char* buf = smem + slot * ST64;
    f.a[0] = lds_b128(buf + ao);
    f.w[0] = rd_g64(buf + wo0, 0);
    f.a[1] = lds_b128(buf + ao + 2048);
    f.w[1] = rd_g64(buf + wo1, 0);
    if (HAS32) f.w[2] = rd_g32(buf + wo2, 0);
  };
  auto mm = [&](const Frag64& f, int s) {
#pragma unroll
    for (int j = 0; j < (HAS32 ? 3 : 2); ++j) {
      acc[0][j] = mfma16(f.w[j], f.a[0], acc[0][j]);
      acc[1][j] = mfma16(f.w[j], f.a[1], acc[1][j]);
    }
    if (QKV) {
#pragma unroll
      for (int mi = 0; mi < 2; ++mi) {
        if (wn == 0) st[mi] = mfma16(ones8(), f.a[mi], st[mi]);
        else st[mi] = mfma16(f.a[mi], f.a[mi], st[mi]);
      }
      if ((s & 1) == wm2) {
#pragma unroll
        for (int j = 0; j < (HAS32 ? 3 : 2); ++j) cs[j] = mfma16(f.w[j], ones8(), cs[j]);
      }
    }
  };
  constexpr int NMAIN = NS - RING + 1;
  static_assert(NMAIN % 2 == 0 && RING == 7, "");
#pragma unroll
  for (int s = 0; s < RING - 1; ++s) issue(s, s);
  Frag64 f0, f1;
  wait_vm<NPW*(RING - 2)>();
  bar();
  STAMP2(QKV ? 0 : (NS == 10 ? 2 : 4), 0);
  rd(f0, 0);
  int slot = 0;
#define STEP(CUR, NXT, S, WAITN, ISSUE)                                   \
  {                                                                       \
    const int nslot = slot == RING - 1 ? 0 : slot + 1;                    \
    wait_vm<WAITN>();                                                     \
    bar();                                                                \
    STAMP2(QKV ? 0 : (NS == 10 ? 2 : 4), (S) + 1);                        \
    if (ISSUE) issue((S) + RING - 1, slot == 0 ? RING - 1 : slot - 1);    \
    rd(NXT, nslot);                                                       \
    __builtin_amdgcn_sched_barrier(0);                                    \
    mm(CUR, S);                                                           \
    __builtin_amdgcn_sched_barrier(0);                                    \
    slot = nslot;                                                         \
  }
#pragma unroll 1
  for (int s = 0; s < NMAIN; s += 2) {
    STEP(f0, f1, s, NPW*(RING - 3), true)
    STEP(f1, f0, s + 1, NPW*(RING - 3), true)
  }
  STEP(f0, f1, NS - 6, NPW * 4, false)
  STEP(f1, f0, NS - 5, NPW * 3, false)
  STEP(f0, f1, NS - 4, NPW * 2, false)
  STEP(f1, f0, NS - 3, NPW * 1, false)
  STEP(f0, f1, NS - 2, 0, false)
  mm(f1, NS - 1);
#undef STEP
  bar();
  f32x4* xs = (f32x4*)smem;
#pragma unroll
  for (int j = 0; j < 3; ++j) {
    f32x4 t0 = acc[0][j], t1 = acc[1][j];
    acc[0][j] = kh ? t1 : t0;
    acc[1][j] = kh ? t0 : t1;
    if (HAS32 || j < 2) *(LDS_AS f32x4*)&xs[(wave * 3 + j) * 64 + lane] = acc[1][j];
  }
  lds_fence();
  bar();
#pragma unroll
  for (int j = 0; j < 3; ++j) {
    if (HAS32 || j < 2) out[j] = acc[0][j] + *(LDS_AS f32x4*)&xs[((wave ^ 1) * 3 + j) * 64 + lane];
    else out[j] = acc[0][j];
  }
}

template <int NS, int RING, bool QKV>
__device__ __forceinline__ void gemm64(char* smem, const u16* A, int lda, const u16* W, int ldw, int ct, int wave, int lane,
                                       f32x4 (&acc)[3], f32x4 (&st)[2], f32x4 (&cs)[3]) {
  const int c64 = (ct & 1) ? 32 : 0, c32 = (ct & 1) ? 0 : 64;
  const unsigned va = voff_a(wave * 8, lane, lda);
  const unsigned v64 = voff_g64(wave, lane, ldw, c64), v32 = voff_g32(wave & 3, lane, ldw, c32);
  const char* ab = (const char*)A;
  const char* wb = (const char*)W;
  if (ct == 6) gemm64_loop<NS, RING, 2, false, QKV>(smem, ab, wb, ldw * 2, va, v64, v32, wave, lane, acc, st, cs);
  else if (wave < 4) gemm64_loop<NS, RING, 3, true, QKV>(smem, ab, wb, ldw * 2, va, v64, v32, wave, lane, acc, st, cs);
  else gemm64_loop<NS, RING, 2, true, QKV>(smem, ab, wb, ldw * 2, va, v64, v32, wave, lane, acc, st, cs);
}
__device__ __forceinline__ int acc_col(int ct, int wn, int j) {
  return j < 2 ? ((ct & 1) ? 32 : 0) + (wn * 2 + j) * 16 : ((ct & 1) ? 0 : 64) + wn * 16;
}

constexpr int K1_RING = 7;
constexpr int K1_LDS = 145 * 1024;

__global__ __launch_bounds__(512, 2) void k1_qkv(const u16* __restrict__ x, const u16* __restrict__ wq,
                                                 const u16* __restrict__ wk, const u16* __restrict__ wv,
                                                 char* __restrict__ ws) {
  extern __shared__ __attribute__((aligned(16))) char smem[];
  const int tid = threadIdx.x, lane = tid & 63;
  const int wave = __builtin_amdgcn_readfirstlane(tid >> 6);
  const int wm = wave & 3, wn = wave >> 2;
  const int b = blockIdx.x;
  const int xcd = b & 7, idx = b >> 3;
  const int rb = xcd * 4 + (idx & 3);
  const int ct = idx >> 2;
  const int row0 = rb * 64;
  const int g = lane >> 4, q = (lane & 15) >> 2, p = lane & 3;
  const int arow = wm * 16 + (lane & 15);

  f32x4 acc[3] = {}, st2[2] = {}, cs[3] = {};
  f32x4& st = st2[0];
  float* scr = (float*)(smem + 142 * 1024);

  STAMP(ws, 0, 0);
  if (PF) pf_regions<1>((const char*)(x + (size_t)xcd * 256 * D_), 256 * D_ * 2 / 128, (const char*)wq, D_ * D_ * 2 / 128, idx, 32, wave,
                lane, lds_addr(smem) + 143360);
  if (ct < 7) {
    gemm64<10, K1_RING, true>(smem, x + (size_t)row0 * D_, D_, wq + ct * 96, D_, ct, wave, lane, acc, st2, cs);
    STAMP(ws, 0, 1);
  } else {
    constexpr int WOFF = 80 * 1024;
    {
      const unsigned vo = (unsigned)((lane >> 3) * D_ + (((lane & 7) ^ (lane >> 3)) << 3)) * 2u;
      const char* ab = (const char*)(x + (size_t)(row0 + wave * 8) * D_);
      const unsigned l0 = lds_addr(smem);
#pragma unroll
      for (int s = 0; s < 10; ++s)
        glds16s(vo, ab + s * 128, l0 + s * 8192 + wave * 1024);
    }
    u32x2 wv_[13];
#pragma unroll
    for (int it = 0; it < 13; ++it) {
      int e = min(it * 512 + tid, 6399);
      int k = e / 10, c4 = e - k * 10;
      const u16* src = (c4 < 5) ? (wk + k * 20 + c4 * 4) : (wv + k * 20 + (c4 - 5) * 4);
      wv_[it] = *(const u32x2*)src;
    }
#pragma unroll
    for (int it = 0; it < 13; ++it) {
      int e = min(it * 512 + tid, 6399);
      int k = e / 10, c4 = e - k * 10;
      int kb = k >> 3, cb = c4 >> 2;
      int rp = (k & 7) ^ ((kb & 1) << 2);
      *(LDS_AS u32x2*)(smem + WOFF + (kb * 3 + cb) * 256 + rp * 32 + (c4 & 3) * 8) = wv_[it];
    }
    wait_vm<0>();
    __syncthreads();
    if (wn == 0 || true) {
      const int boff = g * 3 * 256 + q * 32 + p * 8;
      const int b0 = WOFF + boff + (g & 1) * 128, b1 = WOFF + boff + (1 - (g & 1)) * 128;
#pragma unroll
      for (int kstep = 0; kstep < 20; ++kstep) {
        int s = kstep >> 1, ks = kstep & 1;
        int a_off = s * 8192 + arow * 128 + (((ks * 4 + g) ^ (arow & 7)) << 4);
        bf16x8 a = lds_b128(smem + a_off);
        if (wn == 0) {
#pragma unroll
          for (int j = 0; j < 3; ++j) {
            bf16x8 w = lds_tr_pair(smem + b0 + kstep * 4 * 3 * 256 + j * 256, smem + b1 + kstep * 4 * 3 * 256 + j * 256);
            acc[j] = mfma16(w, a, acc[j]);
          }
          st = mfma16(ones8(), a, st);
        } else {
          st = mfma16(a, a, st);
          if ((kstep & 3) == wm) {
#pragma unroll
            for (int j = 0; j < 3; ++j) {
              bf16x8 w = lds_tr_pair(smem + b0 + kstep * 4 * 3 * 256 + j * 256, smem + b1 + kstep * 4 * 3 * 256 + j * 256);
              cs[j] = mfma16(w, ones8(), cs[j]);
            }
          }
        }
      }
    }
  }
  if (ct < 7) {
    const int kh = wave & 1, r32 = (wave & 2) * 16;
#pragma unroll
    for (int mi = 0; mi < 2; ++mi) {
      if (wn == 0) {
        if (g == 0) scr[kh * 64 + r32 + mi * 16 + lane] = st2[mi][0];
      } else {
        if (g == q) scr[128 + kh * 64 + r32 + mi * 16 + (lane & 15)] = st2[mi][p == 0 ? 0 : p == 1 ? 1 : p == 2 ? 2 : 3];
      }
    }
  } else if (wn == 0) {
    if (g == 0) { scr[wm * 16 + lane] = st[0]; scr[64 + wm * 16 + lane] = 0.f; }
  } else {
    if (g == q) {
      scr[128 + wm * 16 + (lane & 15)] = st[p == 0 ? 0 : p == 1 ? 1 : p == 2 ? 2 : 3];
      scr[192 + wm * 16 + (lane & 15)] = 0.f;
    }
  }
  if ((lane & 15) == 0) {
    if (ct < 7) {
#pragma unroll
      for (int j = 0; j < 3; ++j) *(LDS_AS f32x4*)(&scr[256 + wm * 96 + acc_col(ct, wn, j) + g * 4]) = cs[j];
    } else if (wn == 1) {
#pragma unroll
      for (int j = 0; j < 3; ++j) *(LDS_AS f32x4*)(&scr[256 + wm * 96 + j * 16 + g * 4]) = cs[j];
    }
  }
  __syncthreads();
  const float rsum = scr[arow] + scr[64 + arow], rsq = scr[128 + arow] + scr[192 + arow];
  const float mean = rsum * (1.0f / D_);
  const float var = fmaxf(rsq * (1.0f / D_) - mean * mean, 0.0f);
  const float rstd = rsqrtf(var + LN_EPS);
  const int row = row0 + arow;
  if (ct < 7) {
    u16* Q = (u16*)(ws + WS_Q);
    const int n0 = ct * 96;
#pragma unroll
    for (int j = 0; j < 3; ++j) {
      if (ct == 6 && j == 2) continue;
      int nl = acc_col(ct, wn, j) + g * 4;
      f32x4 c = *(LDS_AS f32x4*)&scr[256 + nl];
      c += *(LDS_AS f32x4*)&scr[256 + 96 + nl];
      c += *(LDS_AS f32x4*)&scr[256 + 192 + nl];
      c += *(LDS_AS f32x4*)&scr[256 + 288 + nl];
      float v[4];
#pragma unroll
      for (int r = 0; r < 4; ++r) v[r] = (acc[j][r] - mean * c[r]) * (rstd * QSCALE);
      u32x2 o = {pk_bf16(v[0], v[1]), pk_bf16(v[2], v[3])};
      st_out(Q + (size_t)row * D_ + n0 + nl, o);
    }
  } else if (wn == 0) {
    char* kv = ws + WS_KV + (size_t)(row >> 5) * KVT;
    const int kk = row & 31;
    float ksq = 0.f;
#pragma unroll
    for (int j = 0; j < 3; ++j) {
      int nl = j * 16 + g * 4;
      if (nl >= 40) continue;
      f32x4 c = *(LDS_AS f32x4*)&scr[256 + nl];
      c += *(LDS_AS f32x4*)&scr[256 + 96 + nl];
      c += *(LDS_AS f32x4*)&scr[256 + 192 + nl];
      c += *(LDS_AS f32x4*)&scr[256 + 288 + nl];
      float v[4];
#pragma unroll
      for (int r = 0; r < 4; ++r) v[r] = (acc[j][r] - mean * c[r]) * rstd;
      u32x2 o = {pk_bf16(v[0], v[1]), pk_bf16(v[2], v[3])};
      if (nl < 20) {
        float a0 = bf_lo(o[0]), a1 = bf_hi(o[0]), a2 = bf_lo(o[1]), a3 = bf_hi(o[1]);
        ksq += a0 * a0 + a1 * a1 + a2 * a2 + a3 * a3;
        if (nl < 16) {
          *(u32x2*)(kv + ((nl >> 3) * 32 + kk) * 16 + (nl & 7) * 2) = o;
        } else {
          *(u32x2*)(kv + 1024 + kk * 8) = o;
          u32x2 cst = {0x3f803f80u, 0x00003f80u};
          *(u32x2*)(kv + 1024 + 256 + kk * 8) = cst;
        }
      } else {
        int d = nl - 20;
        int s = kk >> 4, k16 = kk & 15;
        int h = (k16 >> 2) & 1, jj = ((k16 >> 3) << 2) | (k16 & 3);
        u16* vt = (u16*)(kv + 1536) + ((s * 2 + h) * 21 + d) * 8 + jj;
        vt[0] = (u16)(o[0] & 0xffff);
        vt[8] = (u16)(o[0] >> 16);
        vt[16] = (u16)(o[1] & 0xffff);
        vt[24] = (u16)(o[1] >> 16);
        if (d == 16) vt[32] = (u16)0x3f80;
      }
    }
    ksq += __shfl_xor(ksq, 16);
    ksq += __shfl_xor(ksq, 32);
#pragma unroll
    for (int o = 1; o < 16; o <<= 1) ksq = fmaxf(ksq, __shfl_xor(ksq, o));
    if (lane == 0) ((float*)(ws + WS_KMAX))[rb * 4 + wm] = ksq;
  }
  STAMP(ws, 0, 2);
}

constexpr int AT_STAGE = 8 * KVT;
constexpr int AT_LDS = 4 * AT_STAGE;

__device__ __forceinline__ void split3(float m, unsigned& w0, unsigned& w1) {
  float a = -m;
  unsigned ua = __builtin_bit_cast(unsigned, a) & 0xffff0000u;
  float r1 = a - __builtin_bit_cast(float, ua);
  unsigned ub = __builtin_bit_cast(unsigned, r1) & 0xffff0000u;
  float r2 = r1 - __builtin_bit_cast(float, ub);
  unsigned uc = __builtin_bit_cast(unsigned, r2) & 0xffff0000u;
  w0 = (ua >> 16) | ub;
  w1 = uc >> 16;
}

struct KFrag { bf16x8 k0; s16x4 k1; };
struct VFrag { bf16x8 v0, v1; };
__device__ __forceinline__ void ld_k(KFrag& f, const char* kt, int lane) {
  f.k0 = lds_b128(kt + lane * 16);
  f.k1 = *(const LDS_AS s16x4*)(kt + 1024 + lane * 8);
}
__device__ __forceinline__ void ld_v(VFrag& f, const char* kt, int voff) {
  f.v0 = lds_b128(kt + 1536 + voff);
  f.v1 = lds_b128(kt + 1536 + 672 + voff);
}
__device__ __forceinline__ f32x16 qk(const KFrag& k, bf16x8 q0, s16x4 q1) {
  f32x16 z = {};
  f32x16 s = __builtin_amdgcn_mfma_f32_32x32x16_bf16(k.k0, q0, z, 0, 0, 0);
  return __builtin_amdgcn_mfma_f32_32x32x8bf16_1k(k.k1, q1, s, 0, 0, 0);
}
#define SB() __builtin_amdgcn_sched_barrier(0)
#define EX(i) __builtin_amdgcn_exp2f(sc[i])
__device__ __forceinline__ void attn_unit(const f32x16& sc, const VFrag& v, f32x16& o, f32x16& sn, const KFrag& k, bf16x8 q0,
                                          s16x4 q1) {
  u32x4 p0, p1;
  f32x16 z = {};
  float e0 = EX(0), e1 = EX(1), e2 = EX(2), e3 = EX(3);
  SB();
  sn = __builtin_amdgcn_mfma_f32_32x32x16_bf16(k.k0, q0, z, 0, 0, 0);
  SB();
  p0[0] = pk_bf16(e0, e1);
  p0[1] = pk_bf16(e2, e3);
  p0[2] = pk_bf16(EX(4), EX(5));
  p0[3] = pk_bf16(EX(6), EX(7));
  SB();
  o = __builtin_amdgcn_mfma_f32_32x32x16_bf16(v.v0, __builtin_bit_cast(bf16x8, p0), o, 0, 0, 0);
  SB();
  e0 = EX(8), e1 = EX(9), e2 = EX(10), e3 = EX(11);
  SB();
  sn = __builtin_amdgcn_mfma_f32_32x32x8bf16_1k(k.k1, q1, sn, 0, 0, 0);
  SB();
  p1[0] = pk_bf16(e0, e1);
  p1[1] = pk_bf16(e2, e3);
  p1[2] = pk_bf16(EX(12), EX(13));
  p1[3] = pk_bf16(EX(14), EX(15));
  SB();
  o = __builtin_amdgcn_mfma_f32_32x32x16_bf16(v.v1, __builtin_bit_cast(bf16x8, p1), o, 0, 0, 0);
  SB();
}

__global__ __launch_bounds__(512, 2) void k2_attn(char* __restrict__ ws, const char* __restrict__ wo,
                                                  const char* __restrict__ wg, const char* __restrict__ wu,
                                                  const char* __restrict__ wd) {
  extern __shared__ __attribute__((aligned(16))) char smem[];
  const int tid = threadIdx.x, lane = tid & 63;
  const int wave = __builtin_amdgcn_readfirstlane(tid >> 6);
  const int grp = wave >> 2, wq = wave & 3;
  const int r = lane & 31, h = lane >> 5;
  const char* kvg = ws + WS_KV;
  const int pos0 = blockIdx.x * 8 + wq * 2;

  unsigned kvo[6];
#pragma unroll
  for (int it = 0; it < 6; ++it) {
    int c = it * 512 + wave * 64 + lane;
    int gg = c >= 1440;
    kvo[it] = (unsigned)(gg * 4 * AT_STAGE + (c - gg * 1440) * 16);
  }
  const unsigned l0 = lds_addr(smem);
  auto issue = [&](int st, int buf) {
#pragma unroll
    for (int it = 0; it < 6; ++it) {
      if (it < 5 || wave < 5) glds16s(kvo[it], kvg + (size_t)st * AT_STAGE, l0 + buf * 2 * AT_STAGE + (it * 512 + wave * 64) * 16);
    }
  };
  STAMP(ws, 1, 0);
  issue(0, 0);

  issue(1, 1);

  float kmax2;
  {
    const float* km = (const float*)(ws + WS_KMAX);
    float m = fmaxf(km[lane], km[lane + 64]);
#pragma unroll
    for (int o = 1; o < 64; o <<= 1) m = fmaxf(m, __shfl_xor(m, o));
    kmax2 = m;
  }
  bf16x8 q0[2];
  s16x4 q1[2];
  u32x2 qtail[2];
  float mshift[2];
  const u16* Qs = (const u16*)(ws + WS_Q);
#pragma unroll
  for (int t = 0; t < 2; ++t) {
    const u16* qp = Qs + (size_t)(pos0 + t) * D_ + r * 20;
    u32x2 a = *(const u32x2*)(qp + 8 * h), b2 = *(const u32x2*)(qp + 8 * h + 4);
    u32x4 qq = {a[0], a[1], b2[0], b2[1]};
    q0[t] = __builtin_bit_cast(bf16x8, qq);
    u32x2 tl = *(const u32x2*)(qp + 16);
    qtail[t] = tl;
    float ss = 0.f;
#pragma unroll
    for (int i = 0; i < 4; ++i) { float x0 = bf_lo(qq[i]), x1 = bf_hi(qq[i]); ss += x0 * x0 + x1 * x1; }
    if (h == 0) {
#pragma unroll
      for (int i = 0; i < 2; ++i) { float x0 = bf_lo(tl[i]), x1 = bf_hi(tl[i]); ss += x0 * x0 + x1 * x1; }
    }
    ss += __shfl_xor(ss, 32);
    mshift[t] = sqrtf(ss * kmax2) * 1.0001f;
  }

  const int voff = (h * 21 + (r < 21 ? r : 20)) * 16;
  f32x16 o[2];
  float* xch = (float*)smem;
  int* flag = (int*)(smem + AT_LDS + 512);

  for (int attempt = 0;; ++attempt) {
#pragma unroll
    for (int t = 0; t < 2; ++t) {
      unsigned w0, w1;
      split3(mshift[t], w0, w1);
      u32x2 f = (h == 0) ? qtail[t] : u32x2{w0, w1};
      q1[t] = __builtin_bit_cast(s16x4, f);
#pragma unroll
      for (int i = 0; i < 16; ++i) o[t][i] = 0.f;
    }
    if (attempt) { issue(0, 0); issue(1, 1); }
    STAMP(ws, 1, 1);
    wait_vm<0>();
    bar();
    KFrag kc, kn;
    VFrag vc, vn;
    {
      const char* kb = smem + grp * AT_STAGE;
      ld_k(kc, kb, lane);
      ld_v(vc, kb, voff);
    }
    f32x16 sa = qk(kc, q0[0], q1[0]), sb;
    if (attempt == 0 && wave < 4) {
      const char* wb = wave == 0 ? wo : wave == 1 ? wg : wave == 2 ? wu : wd;
      const int per = wave == 0 ? 25 : 60;
      pf_line(wb + (size_t)(blockIdx.x * per + min(lane, per - 1)) * 128, l0 + AT_LDS + 768);
    }
#pragma unroll 1
    for (int st = 0; st < 4; ++st) {
      const char* kb = smem + (st & 1) * 2 * AT_STAGE + grp * AT_STAGE;
      const char* kbn = smem + ((st + 1) & 1) * 2 * AT_STAGE + grp * AT_STAGE;
#pragma unroll
      for (int kt = 0; kt < 8; ++kt) {
        if (kt == 7) {
          wait_vm<0>();
          lds_fence();
          bar();
          if (st < 2) issue(st + 2, st & 1);
        }
        const char* nx = kt < 7 ? kb + (kt + 1) * KVT : kbn;
        ld_k(kn, nx, lane);
        ld_v(vn, nx, voff);
        SB();
        attn_unit(sa, vc, o[0], sb, kc, q0[1], q1[1]);
        attn_unit(sb, vc, o[1], sa, kn, q0[0], q1[0]);
        kc = kn;
        vc = vn;
      }
    }
    __syncthreads();
    STAMP(ws, 1, 2);
    if (grp == 1) {
#pragma unroll
      for (int t = 0; t < 2; ++t)
#pragma unroll
        for (int i = 0; i < 12; ++i) xch[((wq * 2 + t) * 12 + i) * 64 + lane] = o[t][i];
    }
    if (tid == 0) *flag = 0;
    __syncthreads();
    bool bad = false;
    float linv[2];
    if (grp == 0) {
#pragma unroll
      for (int t = 0; t < 2; ++t) {
#pragma unroll
        for (int i = 0; i < 12; ++i) o[t][i] += xch[((wq * 2 + t) * 12 + i) * 64 + lane];
        float l = __shfl(o[t][8], 32 + r);
        bad |= !(l >= 5.421010862427522e-20f);
        linv[t] = 1.0f / l;
      }
      if (attempt == 0 && bad) *flag = 1;
    }
    __syncthreads();
    if (attempt == 1 || *flag == 0) {
      if (grp == 0) {
        u16* O = (u16*)(ws + WS_O);
#pragma unroll
        for (int t = 0; t < 2; ++t) {
          u16* op = O + (size_t)(pos0 + t) * D_ + r * 20 + 4 * h;
#pragma unroll
          for (int gq = 0; gq < 3; ++gq) {
            if (gq == 2 && h == 1) continue;
            u32x2 v = {pk_bf16(o[t][4 * gq] * linv[t], o[t][4 * gq + 1] * linv[t]),
                       pk_bf16(o[t][4 * gq + 2] * linv[t], o[t][4 * gq + 3] * linv[t])};
            st_out(op + 8 * gq, v);
          }
        }
      }
      break;
    }
    __syncthreads();
#pragma unroll 1
    for (int t = 0; t < 2; ++t) {
      const u16* qp = Qs + (size_t)(pos0 + t) * D_ + r * 20;
      float qf[20];
#pragma unroll
      for (int i = 0; i < 10; ++i) { unsigned u = *(const unsigned*)(qp + 2 * i); qf[2 * i] = bf_lo(u); qf[2 * i + 1] = bf_hi(u); }
      float mx = -INFINITY;
#pragma unroll 1
      for (int kk = 0; kk < 512; ++kk) {
        int key = grp * 1024 + h * 512 + kk;
        const char* t0 = kvg + (size_t)(key >> 5) * KVT;
        int kr = key & 31;
        u32x4 a = *(const u32x4*)(t0 + kr * 16), b2 = *(const u32x4*)(t0 + 512 + kr * 16);
        u32x2 c = *(const u32x2*)(t0 + 1024 + kr * 8);
        float sacc = 0.f;
#pragma unroll
        for (int i = 0; i < 4; ++i) {
          sacc += qf[2 * i] * bf_lo(a[i]) + qf[2 * i + 1] * bf_hi(a[i]);
          sacc += qf[8 + 2 * i] * bf_lo(b2[i]) + qf[9 + 2 * i] * bf_hi(b2[i]);
        }
        sacc += qf[16] * bf_lo(c[0]) + qf[17] * bf_hi(c[0]) + qf[18] * bf_lo(c[1]) + qf[19] * bf_hi(c[1]);
        mx = fmaxf(mx, sacc);
      }
      mx = fmaxf(mx, __shfl_xor(mx, 32));
      if (h == 0) xch[(grp * 8 + wq * 2 + t) * 32 + r] = mx;
    }
    __syncthreads();
#pragma unroll
    for (int t = 0; t < 2; ++t) mshift[t] = fmaxf(xch[(wq * 2 + t) * 32 + r], xch[(8 + wq * 2 + t) * 32 + r]);
    __syncthreads();
  }
  STAMP(ws, 1, 3);
}

constexpr int G2_RING = 7;
constexpr int G2_LDS = G2_RING * ST64 + 256;

template <int K, bool STATS>
__global__ __launch_bounds__(512, 2) void k_gemm96(const u16* __restrict__ A, const u16* __restrict__ W,
                                                   const u16* __restrict__ res, u16* __restrict__ out,
                                                   float* __restrict__ stats, char* __restrict__ ws) {
  extern __shared__ __attribute__((aligned(16))) char smem[];
  const int tid = threadIdx.x, lane = tid & 63;
  const int wave = __builtin_amdgcn_readfirstlane(tid >> 6);
  const int wm = wave & 3, wn = wave >> 2;
  const int b = blockIdx.x;
  const int xcd = b & 7, idx = b >> 3;
  const int rb = xcd * 4 + (idx & 3), ct = idx >> 2;
  const int row0 = rb * 64, n0 = ct * 96;
  const int g = lane >> 4;
  const int arow = wm * 16 + (lane & 15);
  const int row = row0 + arow;
  const int nj = (ct == 6) ? 2 : 3;
  STAMP(ws, STATS ? 2 : 4, 0);
  if (PF) pf_regions<(K > 640 ? 2 : 1)>((const char*)(A + (size_t)xcd * 256 * K), 256 * K * 2 / 128, (const char*)W, K * D_ * 2 / 128, idx,
                                28, wave, lane, lds_addr(smem) + G2_RING * ST64);

  u32x2 rv[3];
#pragma unroll
  for (int j = 0; j < 3; ++j) {
    int c = (j < nj) ? acc_col(ct, wn, j) : 0;
    rv[j] = *(const u32x2*)(res + (size_t)row * D_ + n0 + c + g * 4);
  }
  f32x4 acc[3] = {}, st[2] = {}, cs[3] = {};
  gemm64<K / 64, G2_RING, false>(smem, A + (size_t)row0 * K, K, W + n0, D_, ct, wave, lane, acc, st, cs);
  STAMP(ws, STATS ? 2 : 4, 1);

  float sum = 0.f;
  float hv[3][4];
#pragma unroll
  for (int j = 0; j < 3; ++j) {
    if (j < nj) {
      float v0 = acc[j][0] + bf_lo(rv[j][0]), v1 = acc[j][1] + bf_hi(rv[j][0]);
      float v2 = acc[j][2] + bf_lo(rv[j][1]), v3 = acc[j][3] + bf_hi(rv[j][1]);
      u32x2 o = {pk_bf16(v0, v1), pk_bf16(v2, v3)};
      st_out(out + (size_t)row * D_ + n0 + acc_col(ct, wn, j) + g * 4, o);
      if (STATS) {
        hv[j][0] = bf_lo(o[0]); hv[j][1] = bf_hi(o[0]); hv[j][2] = bf_lo(o[1]); hv[j][3] = bf_hi(o[1]);
        sum += hv[j][0] + hv[j][1] + hv[j][2] + hv[j][3];
      }
    } else if (STATS) {
      hv[j][0] = hv[j][1] = hv[j][2] = hv[j][3] = 0.f;
    }
  }
  if (STATS) {
    float* scr = (float*)smem;
    sum += __shfl_xor(sum, 16);
    sum += __shfl_xor(sum, 32);
    __syncthreads();
    if (g == 0) scr[wn * 64 + arow] = sum;
    __syncthreads();
    const float tot = scr[arow] + scr[64 + arow];
    const float mu = tot * ((ct == 6) ? (1.0f / 64.0f) : (1.0f / 96.0f));
    float m2 = 0.f;
#pragma unroll
    for (int j = 0; j < 3; ++j) {
      if (j < nj) {
#pragma unroll
        for (int r = 0; r < 4; ++r) { float d = hv[j][r] - mu; m2 += d * d; }
      }
    }
    m2 += __shfl_xor(m2, 16);
    m2 += __shfl_xor(m2, 32);
    if (g == 0) scr[128 + wn * 64 + arow] = m2;
    __syncthreads();
    if (wn == 0 && g == 0) {
      f32x2 o = {tot, scr[128 + arow] + scr[192 + arow]};
      *(f32x2*)(stats + ((size_t)row * 8 + ct) * 2) = o;
    }
  }
  STAMP(ws, STATS ? 2 : 4, 2);
}

constexpr int G3_RING = 4;
constexpr int G3_LDS = G3_RING * 40 * 1024;

__global__ __launch_bounds__(512, 2) void k4_ffn_up(const u16* __restrict__ Hh, const u16* __restrict__ wg,
                                                    const u16* __restrict__ wu, const float* __restrict__ stats,
                                                    u16* __restrict__ act,
                                                    char* __restrict__ ws) {
  extern __shared__ __attribute__((aligned(16))) char smem[];
  constexpr int ST = 40 * 1024;
  const int tid = threadIdx.x, lane = tid & 63;
  const int wave = __builtin_amdgcn_readfirstlane(tid >> 6);
  const int wm = wave & 3, wn = wave >> 2;
  const int b = blockIdx.x;
  const int xcd = b & 7, idx = b >> 3;
  const int rb = (xcd & 1) * 8 + (idx & 7);
  const int ct = (xcd >> 1) * 4 + (idx >> 3);
  const int row0 = rb * 128, n0 = ct * 96;
  const int g = lane >> 4;
  STAMP(ws, 3, 0);
  if (PF) {
    const char* hb = (const char*)(Hh + (size_t)(xcd & 1) * 1024 * D_);
    const int wc0 = (xcd >> 1) * 384;
#pragma unroll
    for (int t = 0; t < 2; ++t) {
      int i0 = (wave + t * 8) * 64;
      if (i0 < 560) {
        int l = min(idx * 560 + i0 + lane, 17919);
        int lw = l - 10240;
        int m = lw >= 3840;
        lw -= m * 3840;
        int k = lw / 6, c = lw - k * 6;
        const char* wp = (const char*)((m ? wu : wg) + (size_t)k * F_ + wc0) + c * 128;
        pf_line(l < 10240 ? hb + (size_t)l * 128 : wp, lds_addr(smem) + 3 * ST + ST - 256);
      }
    }
  }

  const f32x4* sp = (const f32x4*)(stats + (size_t)(row0 + (tid & 127)) * 16);
  const f32x4 s0 = sp[0], s1 = sp[1], s2 = sp[2];
  const f32x2 s3 = *(const f32x2*)(sp + 3);
  const int c64 = (ct & 1) ? 32 : 0, c32 = (ct & 1) ? 0 : 64;
  const unsigned va = voff_a(wave * 16, lane, D_);
  const unsigned v64 = voff_g64(wave, lane, F_, c64), v32 = voff_g32(wave & 3, lane, F_, c32);
  const char* abase = (const char*)(Hh + (size_t)row0 * D_);
  const char* gbase = (const char*)(wg + n0);
  const char* ubase = (const char*)(wu + n0);
  const unsigned l0 = lds_addr(smem);
  auto issue = [&](int s, int slot) {
    const unsigned d = l0 + slot * ST;
    const char* gw = gbase + (size_t)s * 64 * F_ * 2;
    const char* uw = ubase + (size_t)s * 64 * F_ * 2;
    glds16s(va, abase + s * 128, d + wave * 2048);
    glds16s(va, abase + s * 128 + 8 * D_ * 2, d + wave * 2048 + 1024);
    glds16s(v64, gw, d + 16384 + wave * 1024);
    glds16s(v64, uw, d + 24576 + wave * 1024);
    glds16s(v32, wave < 4 ? gw : uw, d + 32768 + wave * 1024);
  };
#pragma unroll
  for (int s = 0; s < G3_RING - 1; ++s) issue(s, s);

  const int wo0 = 16384 + roff_g64(wn * 2, lane), wo1 = 16384 + roff_g64(wn * 2 + 1, lane), wo2 = 32768 + roff_g32(wn, lane);
  const int ar0 = wm * 32 + (lane & 15);
  f32x4 ag[2][3] = {}, au[2][3] = {};
  struct Frag { bf16x8 a[2], g[3], u[3]; };
  const int ao0 = ar0 * 128 + ((g ^ (ar0 & 7)) << 4), ao1 = ar0 * 128 + (((4 + g) ^ (ar0 & 7)) << 4);
  auto rd = [&](Frag& f, const char* buf, int ks) {
    f.a[0] = lds_b128(buf + (ks ? ao1 : ao0));
    f.g[0] = rd_g64(buf + wo0, ks);
    f.u[0] = rd_g64(buf + wo0 + 8192, ks);
    f.a[1] = lds_b128(buf + (ks ? ao1 : ao0) + 2048);
    f.g[1] = rd_g64(buf + wo1, ks);
    f.u[1] = rd_g64(buf + wo1 + 8192, ks);
    f.g[2] = rd_g32(buf + wo2, ks);
    f.u[2] = rd_g32(buf + wo2 + 4096, ks);
  };
  f32x4 cg[3] = {}, cu[3] = {};
  auto mm = [&](const Frag& f, int step) {
#pragma unroll
    for (int j = 0; j < 3; ++j)
#pragma unroll
      for (int mi = 0; mi < 2; ++mi) {
        ag[mi][j] = mfma16(f.g[j], f.a[mi], ag[mi][j]);
        au[mi][j] = mfma16(f.u[j], f.a[mi], au[mi][j]);
      }
    if ((step & 3) == wm) {
#pragma unroll
      for (int j = 0; j < 3; ++j) {
        cg[j] = mfma16(f.g[j], ones8(), cg[j]);
        cu[j] = mfma16(f.u[j], ones8(), cu[j]);
      }
    }
  };
  Frag f0, f1;
  wait_vm<10>();
  bar();
  STAMP2(3, 0);
  rd(f0, smem, 0);
#pragma unroll
  for (int s = 0; s < 10; ++s) {
    const char* buf = smem + (s % G3_RING) * ST;
    rd(f1, buf, 1);
    __builtin_amdgcn_sched_barrier(0);
    mm(f0, s * 2);
    __builtin_amdgcn_sched_barrier(0);
    if (s < 9) {
      if (s < 8) wait_vm<5>();
      else wait_vm<0>();
      bar();
      STAMP2(3, s + 1);
      if (s + 3 < 10) issue(s + 3, (s + 3) % G3_RING);
      rd(f0, smem + ((s + 1) % G3_RING) * ST, 0);
      __builtin_amdgcn_sched_barrier(0);
    }
    mm(f1, s * 2 + 1);
    __builtin_amdgcn_sched_barrier(0);
  }
  bar();
  STAMP(ws, 3, 1);
  float* scr = (float*)smem;
  float mean_r = 0.f, rstd_r = 0.f;
  if (tid < 128) {
    float tot = s0[0] + s0[2] + s1[0] + s1[2] + s2[0] + s2[2] + s3[0];
    float mean = tot * (1.0f / D_);
    float m2 = s0[1] + s0[3] + s1[1] + s1[3] + s2[1] + s2[3] + s3[1];
    float c = 0.f, d;
    d = s0[0] * (1.f / 96.f) - mean; c += d * d; d = s0[2] * (1.f / 96.f) - mean; c += d * d;
    d = s1[0] * (1.f / 96.f) - mean; c += d * d; d = s1[2] * (1.f / 96.f) - mean; c += d * d;
    d = s2[0] * (1.f / 96.f) - mean; c += d * d; d = s2[2] * (1.f / 96.f) - mean; c += d * d;
    d = s3[0] * (1.f / 64.f) - mean;
    float var = (m2 + 96.f * c + 64.f * d * d) * (1.0f / D_);
    mean_r = mean;
    rstd_r = rsqrtf(var + LN_EPS);
  }

  if (tid < 128) { scr[tid * 2] = mean_r; scr[tid * 2 + 1] = rstd_r; }
  if ((lane & 15) == 0) {
#pragma unroll
    for (int j = 0; j < 3; ++j) {
      *(LDS_AS f32x4*)&scr[256 + wm * 192 + acc_col(ct, wn, j) + g * 4] = cg[j];
      *(LDS_AS f32x4*)&scr[256 + wm * 192 + 96 + acc_col(ct, wn, j) + g * 4] = cu[j];
    }
  }
  __syncthreads();
#pragma unroll
  for (int j = 0; j < 3; ++j) {
    const int nl = 256 + acc_col(ct, wn, j) + g * 4;
    cg[j] = *(LDS_AS f32x4*)&scr[nl] + *(LDS_AS f32x4*)&scr[nl + 192] + *(LDS_AS f32x4*)&scr[nl + 384] + *(LDS_AS f32x4*)&scr[nl + 576];
    cu[j] = *(LDS_AS f32x4*)&scr[nl + 96] + *(LDS_AS f32x4*)&scr[nl + 288] + *(LDS_AS f32x4*)&scr[nl + 480] + *(LDS_AS f32x4*)&scr[nl + 672];
  }
#pragma unroll
  for (int mi = 0; mi < 2; ++mi) {
    const int ar = ar0 + mi * 16;
    const f32x2 ms = *(LDS_AS f32x2*)&scr[ar * 2];
    const float rstd = ms[1], nm = -ms[0] * ms[1];
#pragma unroll
    for (int j = 0; j < 3; ++j) {
      const int n = n0 + acc_col(ct, wn, j) + g * 4;
      float o[4];
#pragma unroll
      for (int r = 0; r < 4; ++r) {
        float gv = ag[mi][j][r] * rstd + nm * cg[j][r];
        float uv = au[mi][j][r] * rstd + nm * cu[j][r];
        float t = gv * gv * (0.044715f * 1.5957691216057308f * 1.4426950408889634f) + (1.5957691216057308f * 1.4426950408889634f);
        float e = __builtin_amdgcn_exp2f(-gv * t);
        o[r] = gv * uv * __builtin_amdgcn_rcpf(1.0f + e);
      }
      u32x2 v = {pk_bf16(o[0], o[1]), pk_bf16(o[2], o[3])};
      st_out(act + (size_t)(row0 + ar) * F_ + n, v);
    }
  }
  STAMP(ws, 3, 2);
}

extern "C" void kernel_launch(void* const* d_in, const int* in_sizes, int n_in, void* d_out, int out_size,
                              void* d_ws, size_t ws_size, hipStream_t stream) {
  const u16* x = (const u16*)d_in[0];
  const u16* wq = (const u16*)d_in[1];
  const u16* wk = (const u16*)d_in[2];
  const u16* wv = (const u16*)d_in[3];
  const u16* wo = (const u16*)d_in[4];
  const u16* wg = (const u16*)d_in[6];
  const u16* wu = (const u16*)d_in[7];
  const u16* wd = (const u16*)d_in[8];
  char* ws = (char*)d_ws;
  u16* out = (u16*)d_out;

  (void)hipFuncSetAttribute((const void*)k1_qkv, hipFuncAttributeMaxDynamicSharedMemorySize, K1_LDS);
  (void)hipFuncSetAttribute((const void*)k2_attn, hipFuncAttributeMaxDynamicSharedMemorySize, AT_LDS + 1024);
  (void)hipFuncSetAttribute((const void*)k_gemm96<640, true>, hipFuncAttributeMaxDynamicSharedMemorySize, G2_LDS);
  (void)hipFuncSetAttribute((const void*)k_gemm96<1536, false>, hipFuncAttributeMaxDynamicSharedMemorySize, G2_LDS);
  (void)hipFuncSetAttribute((const void*)k4_ffn_up, hipFuncAttributeMaxDynamicSharedMemorySize, G3_LDS);

  k1_qkv<<<256, 512, K1_LDS, stream>>>(x, wq, wk, wv, ws);
  k2_attn<<<256, 512, AT_LDS + 1024, stream>>>(ws, (const char*)wo, (const char*)wg, (const char*)wu, (const char*)wd);
  k_gemm96<640, true><<<224, 512, G2_LDS, stream>>>((const u16*)(ws + WS_O), wo, x, (u16*)(ws + WS_H),
                                                    (float*)(ws + WS_STAT), ws);
  k4_ffn_up<<<256, 512, G3_LDS, stream>>>((const u16*)(ws + WS_H), wg, wu, (const float*)(ws + WS_STAT),
                                          (u16*)(ws + WS_ACT), ws);
  k_gemm96<1536, false><<<224, 512, G2_LDS, stream>>>((const u16*)(ws + WS_ACT), wd, (const u16*)(ws + WS_H), out,
                                                      (float*)(ws + WS_STAT), ws);
}
```
